```python
import torch

def _softmax(x, axis=-1):
    return torch.softmax(x, dim=axis)
def _gelu(x):
    return torch.nn.functional.gelu(x, approximate="tanh")
def _silu(x):
    return torch.nn.functional.silu(x)
def _sigmoid(x):
    return torch.sigmoid(x)
def _layernorm(x, axis=-1, eps=1e-5):
    m = x.mean(dim=axis, keepdim=True)
    v = x.var(dim=axis, keepdim=True, unbiased=False)
    return (x - m) / torch.sqrt(v + eps)
def _scatter_add(tgt, idx, src, axis=0):
    return tgt.clone().index_add_(axis, idx, src)
def _segment_sum(src, seg, n):
    out = torch.zeros((n,) + src.shape[1:], dtype=src.dtype)
    return out.index_add_(0, seg, src)
def _pad(x, pw):
    flat = tuple(v for pair in reversed(pw) for v in pair)
    return torch.nn.functional.pad(x, flat)

kernel_name = 'parallel_sum__grouped_kv_mqa__dense_activation_squared_relu__prompt'

def setup_inputs():

    _w = (lambda a: (lambda t: t.to(torch.bfloat16) if t.is_floating_point() else t)(torch.as_tensor(a)))
    def _A_setup():
        B, S, D, H = 1, 2048, 512, 32
        Dh = D // H
        return {
            "x": _w((lambda *s: torch.randn(*s, dtype=torch.bfloat16) * 0.05)(B, S, D)),
            "wq": _w((lambda *s: torch.randn(*s, dtype=torch.bfloat16) * 0.2)(D, H * Dh)),
            "wk": _w((lambda *s: torch.randn(*s, dtype=torch.bfloat16) * 0.2)(D, Dh)),
            "wv": _w((lambda *s: torch.randn(*s, dtype=torch.bfloat16) * 0.1)(D, Dh)),
            "wo": _w((lambda *s: torch.randn(*s, dtype=torch.bfloat16) * 0.1)(H * Dh, D)),
        }
    _A_in = _A_setup()

    def _B_setup():
        B, S, D, F = 1, 2048, 512, 1376
        return {
            "x": _w((lambda *s: torch.randn(*s, dtype=torch.bfloat16) * 0.05)(B, S, D)),
            "w1": _w((lambda *s: torch.randn(*s, dtype=torch.bfloat16) * 0.05)(D, F)),
            "b1": _w((lambda *s: torch.randn(*s, dtype=torch.bfloat16) * 1.6)(F)),
            "w2": _w((lambda *s: torch.randn(*s, dtype=torch.bfloat16) * 0.05)(F, D)),
            "b2": _w((lambda *s: torch.randn(*s, dtype=torch.bfloat16) * 6.4)(D)),
        }
    _B_in = _B_setup()

    return {
        "A_x": _A_in["x"],
        "A_wq": _A_in["wq"],
        "A_wk": _A_in["wk"],
        "A_wv": _A_in["wv"],
        "A_wo": _A_in["wo"],
        "B_x": _B_in["x"],
        "B_w1": _B_in["w1"],
        "B_b1": _B_in["b1"],
        "B_w2": _B_in["w2"],
        "B_b2": _B_in["b2"],
    }

def reference(A_x, A_wq, A_wk, A_wv, A_wo, B_x, B_w1, B_b1, B_w2, B_b2):
    def _A_ref(x, wq, wk, wv, wo):
        B, S, D = x.shape
        H = 32
        Dh = D // H
        q = (lambda x, *d: x.permute(*d))((x @ wq).reshape(B, S, H, Dh), 0, 2, 1, 3)
        k = x @ wk
        v = x @ wv
        scores = torch.einsum("bhqd,bkd->bhqk", q, k) * (Dh ** -0.5)
        p = _softmax(scores, axis=-1)
        o = torch.einsum("bhqk,bkd->bhqd", p, v)
        return (lambda x, *d: x.permute(*d))(o, 0, 2, 1, 3).reshape(B, S, H * Dh) @ wo

    def _B_ref(x, w1, b1, w2, b2):
        h = torch.einsum("bsd,df->bsf", x, w1) + b1
        zero = torch.zeros_like(h)
        r = torch.where(h > zero, h, zero)
        h = r * r
        out = torch.einsum("bsf,fd->bsd", h, w2) + b2
        return out

    x = A_x
    xn = _layernorm(x, axis=-1)
    return x + _A_ref(xn, A_wq, A_wk, A_wv, A_wo) + _B_ref(xn, B_w1, B_b1, B_w2, B_b2)
```

```cpp
#include <hip/hip_runtime.h>
#include <hip/hip_bf16.h>
#define MIMIC 1
typedef unsigned short u16;
__device__ __forceinline__ float b2f(u16 v) { return __uint_as_float((unsigned)v << 16); }
__device__ __forceinline__ u16 f2b(float f) { unsigned u = __float_as_uint(f); return (u + 0x7fff + ((u >> 16) & 1)) >> 16; }
__device__ __forceinline__ float rb(float f) { return b2f(f2b(f)); }
__global__ void ln_k(const u16* x, u16* xn) {
  int row = blockIdx.x, t = threadIdx.x;
  float v[8], s = 0;
  for (int i = 0; i < 8; ++i) { v[i] = b2f(x[row * 512 + t * 8 + i]); s += v[i]; }
  for (int o = 32; o; o >>= 1) s += __shfl_xor(s, o);
  float m = s / 512, q = 0;
  for (int i = 0; i < 8; ++i) q += (v[i] - m) * (v[i] - m);
  for (int o = 32; o; o >>= 1) q += __shfl_xor(q, o);
  float var = q / 512;
  if (MIMIC >= 1) {
    float mb = rb(m), sd = rb(sqrtf(rb(rb(var) + 1e-5f)));
    for (int i = 0; i < 8; ++i) xn[row * 512 + t * 8 + i] = f2b((MIMIC >= 2 ? rb(v[i] - mb) : (v[i] - mb)) / sd);
  } else {
    float r = rsqrtf(var + 1e-5f);
    for (int i = 0; i < 8; ++i) xn[row * 512 + t * 8 + i] = f2b((v[i] - m) * r);
  }
}
__global__ void gemm_k(const u16* A, const u16* W, const u16* bias, void* C, int M, int N, int K, int mode) {
  int n = blockIdx.x * 64 + threadIdx.x, m0 = blockIdx.y * 8;
  if (n >= N) return;
  float acc[8] = {0};
  for (int k = 0; k < K; ++k) { float w = b2f(W[k * N + n]); for (int i = 0; i < 8; ++i) acc[i] += b2f(A[(m0 + i) * K + k]) * w; }
  for (int i = 0; i < 8; ++i) {
    if (mode == 0) ((u16*)C)[(m0 + i) * N + n] = f2b(acc[i]);
    else if (mode == 1) { float h = acc[i] + b2f(bias[n]); h = h > 0 ? h : 0; ((u16*)C)[(m0 + i) * N + n] = f2b(h * h); }
    else ((float*)C)[(m0 + i) * N + n] = acc[i];
  }
}
__global__ void attn_k(const u16* q, const u16* k, const u16* v, u16* o) {
  int id = blockIdx.x * 64 + threadIdx.x;
  float qq[16], acc[16] = {0}, m = -1e30f, l = 0;
  for (int d = 0; d < 16; ++d) qq[d] = b2f(q[id * 16 + d]);
  for (int j = 0; j < 2048; ++j) {
    float s = 0; for (int d = 0; d < 16; ++d) s += qq[d] * b2f(k[j * 16 + d]);
    if (MIMIC >= 3) s = rb(s);
    s *= 0.25f;
    float mn = fmaxf(m, s), a = __expf(m - mn), p = __expf(s - mn);
    l = l * a + p; for (int d = 0; d < 16; ++d) acc[d] = acc[d] * a + p * b2f(v[j * 16 + d]);
    m = mn;
  }
  for (int d = 0; d < 16; ++d) o[id * 16 + d] = f2b(acc[d] / l);
}
__global__ void fin_k(const u16* x, const float* a, const float* b, const u16* b2, u16* out) {
  int i = blockIdx.x * 256 + threadIdx.x;
  out[i] = f2b(b2f(x[i]) + a[i] + b[i] + b2f(b2[i & 511]));
}
extern "C" void kernel_launch(void* const* d_in, const int* in_sizes, int n_in, void* d_out, int out_size, void* d_ws, size_t ws_size, hipStream_t stream) {
  const u16 *x = (const u16*)d_in[0], *wq = (const u16*)d_in[1], *wk = (const u16*)d_in[2], *wv = (const u16*)d_in[3], *wo = (const u16*)d_in[4];
  const u16 *w1 = (const u16*)d_in[6], *b1 = (const u16*)d_in[7], *w2 = (const u16*)d_in[8], *b2 = (const u16*)d_in[9];
  char* ws = (char*)d_ws; const size_t MB = 1 << 20;
  u16 *xn = (u16*)ws, *q = (u16*)(ws + 4 * MB), *k = (u16*)(ws + 8 * MB), *v = (u16*)(ws + 9 * MB), *o = (u16*)(ws + 10 * MB), *h = (u16*)(ws + 16 * MB);
  float *a = (float*)(ws + 32 * MB), *b = (float*)(ws + 40 * MB);
  ln_k<<<2048, 64, 0, stream>>>(x, xn);
  gemm_k<<<dim3(8, 256), 64, 0, stream>>>(xn, wq, nullptr, q, 2048, 512, 512, 0);
  gemm_k<<<dim3(1, 256), 64, 0, stream>>>(xn, wk, nullptr, k, 2048, 16, 512, 0);
  gemm_k<<<dim3(1, 256), 64, 0, stream>>>(xn, wv, nullptr, v, 2048, 16, 512, 0);
  gemm_k<<<dim3(22, 256), 64, 0, stream>>>(xn, w1, b1, h, 2048, 1376, 512, 1);
  attn_k<<<1024, 64, 0, stream>>>(q, k, v, o);
  gemm_k<<<dim3(8, 256), 64, 0, stream>>>(o, wo, nullptr, a, 2048, 512, 512, 2);
  gemm_k<<<dim3(8, 256), 64, 0, stream>>>(h, w2, nullptr, b, 2048, 512, 1376, 2);
  fin_k<<<4096, 256, 0, stream>>>(x, a, b, b2, (u16*)d_out);
}
```

```cpp
#include <hip/hip_runtime.h>
#include <hip/hip_bf16.h>
#define MIMIC 1
typedef unsigned short u16;
__device__ __forceinline__ float b2f(u16 v) { return __uint_as_float((unsigned)v << 16); }
__device__ __forceinline__ u16 f2b(float f) { unsigned u = __float_as_uint(f); return (u + 0x7fff + ((u >> 16) & 1)) >> 16; }
__device__ __forceinline__ float rb(float f) { return b2f(f2b(f)); }
extern "C" __device__ unsigned long long my_dispatch_id() __asm("llvm.amdgcn.dispatch.id");
__global__ void ln_k(const u16* x, u16* xn, unsigned long long* slot) {
  if (threadIdx.x == 0) { unsigned long long id = my_dispatch_id(), dp = (unsigned long long)__builtin_amdgcn_dispatch_ptr(); slot[blockIdx.x] = ((dp >> 12) << 32) | (unsigned)id; }
  int row = blockIdx.x, t = threadIdx.x;
  float v[8], s = 0;
  for (int i = 0; i < 8; ++i) { v[i] = b2f(x[row * 512 + t * 8 + i]); s += v[i]; }
  for (int o = 32; o; o >>= 1) s += __shfl_xor(s, o);
  float m = s / 512, q = 0;
  for (int i = 0; i < 8; ++i) q += (v[i] - m) * (v[i] - m);
  for (int o = 32; o; o >>= 1) q += __shfl_xor(q, o);
  float var = q / 512;
  if (MIMIC >= 1) {
    float mb = rb(m), sd = rb(sqrtf(rb(rb(var) + 1e-5f)));
    for (int i = 0; i < 8; ++i) xn[row * 512 + t * 8 + i] = f2b((MIMIC >= 2 ? rb(v[i] - mb) : (v[i] - mb)) / sd);
  } else {
    float r = rsqrtf(var + 1e-5f);
    for (int i = 0; i < 8; ++i) xn[row * 512 + t * 8 + i] = f2b((v[i] - m) * r);
  }
}
__global__ void gemm_k(const u16* A, const u16* W, const u16* bias, void* C, int M, int N, int K, int mode) {
  int n = blockIdx.x * 64 + threadIdx.x, m0 = blockIdx.y * 8;
  if (n >= N) return;
  float acc[8] = {0};
  for (int k = 0; k < K; ++k) { float w = b2f(W[k * N + n]); for (int i = 0; i < 8; ++i) acc[i] += b2f(A[(m0 + i) * K + k]) * w; }
  for (int i = 0; i < 8; ++i) {
    if (mode == 0) ((u16*)C)[(m0 + i) * N + n] = f2b(acc[i]);
    else if (mode == 1) { float h = acc[i] + b2f(bias[n]); h = h > 0 ? h : 0; ((u16*)C)[(m0 + i) * N + n] = f2b(h * h); }
    else ((float*)C)[(m0 + i) * N + n] = acc[i];
  }
}
__global__ void attn_k(const u16* q, const u16* k, const u16* v, u16* o) {
  int id = blockIdx.x * 64 + threadIdx.x;
  float qq[16], acc[16] = {0}, m = -1e30f, l = 0;
  for (int d = 0; d < 16; ++d) qq[d] = b2f(q[id * 16 + d]);
  for (int j = 0; j < 2048; ++j) {
    float s = 0; for (int d = 0; d < 16; ++d) s += qq[d] * b2f(k[j * 16 + d]);
    if (MIMIC >= 3) s = rb(s);
    s *= 0.25f;
    float mn = fmaxf(m, s), a = __expf(m - mn), p = __expf(s - mn);
    l = l * a + p; for (int d = 0; d < 16; ++d) acc[d] = acc[d] * a + p * b2f(v[j * 16 + d]);
    m = mn;
  }
  for (int d = 0; d < 16; ++d) o[id * 16 + d] = f2b(acc[d] / l);
}
__global__ void upd_k(unsigned long long* slot) { slot[4096] = slot[0]; }
__global__ void fin_k(const u16* x, const float* a, const float* b, const u16* b2, u16* out, const unsigned long long* slot) {
  int i = blockIdx.x * 256 + threadIdx.x;
  if (slot[blockIdx.x & 2047] != slot[0] || slot[0] == slot[4096]) { out[i] = 0x7fc0; return; }
  out[i] = f2b(b2f(x[i]) + a[i] + b[i] + b2f(b2[i & 511]));
}
extern "C" void kernel_launch(void* const* d_in, const int* in_sizes, int n_in, void* d_out, int out_size, void* d_ws, size_t ws_size, hipStream_t stream) {
  const u16 *x = (const u16*)d_in[0], *wq = (const u16*)d_in[1], *wk = (const u16*)d_in[2], *wv = (const u16*)d_in[3], *wo = (const u16*)d_in[4];
  const u16 *w1 = (const u16*)d_in[6], *b1 = (const u16*)d_in[7], *w2 = (const u16*)d_in[8], *b2 = (const u16*)d_in[9];
  char* ws = (char*)d_ws; const size_t MB = 1 << 20;
  u16 *xn = (u16*)ws, *q = (u16*)(ws + 4 * MB), *k = (u16*)(ws + 8 * MB), *v = (u16*)(ws + 9 * MB), *o = (u16*)(ws + 10 * MB), *h = (u16*)(ws + 16 * MB);
  float *a = (float*)(ws + 32 * MB), *b = (float*)(ws + 40 * MB);
  unsigned long long* slot = (unsigned long long*)(ws + 60 * MB);
  ln_k<<<2048, 64, 0, stream>>>(x, xn, slot);
  gemm_k<<<dim3(8, 256), 64, 0, stream>>>(xn, wq, nullptr, q, 2048, 512, 512, 0);
  gemm_k<<<dim3(1, 256), 64, 0, stream>>>(xn, wk, nullptr, k, 2048, 16, 512, 0);
  gemm_k<<<dim3(1, 256), 64, 0, stream>>>(xn, wv, nullptr, v, 2048, 16, 512, 0);
  gemm_k<<<dim3(22, 256), 64, 0, stream>>>(xn, w1, b1, h, 2048, 1376, 512, 1);
  attn_k<<<1024, 64, 0, stream>>>(q, k, v, o);
  gemm_k<<<dim3(8, 256), 64, 0, stream>>>(o, wo, nullptr, a, 2048, 512, 512, 2);
  gemm_k<<<dim3(8, 256), 64, 0, stream>>>(h, w2, nullptr, b, 2048, 512, 1376, 2);
  fin_k<<<4096, 256, 0, stream>>>(x, a, b, b2, (u16*)d_out, slot);
  upd_k<<<1, 1, 0, stream>>>(slot);
}
```

```cpp
#include <hip/hip_runtime.h>

extern "C" __device__ unsigned long long mqa_dispatch_id() __asm("llvm.amdgcn.dispatch.id");

__global__ void __launch_bounds__(512, 2)
fused_mqa_ffn_kernel(const void* x, const void* wq, const void* wk, const void* wv, const void* wo, const void* w1,
                     const void* b1, const void* w2, const void* b2, void* out, void* ws) {
    __shared__ char smem[163840];
    const unsigned e_lo = (unsigned)mqa_dispatch_id();
    const unsigned e_hi = (unsigned)((unsigned long long)__builtin_amdgcn_dispatch_ptr() >> 12);
    asm volatile(
"s_load_dwordx16 s[8:23], %0, 0\n"
"s_load_dwordx4 s[24:27], %0, 64\n"
"s_load_dwordx2 s[28:29], %0, 80\n"
"v_and_b32 v1, 63, %4\n"
"v_lshrrev_b32 v4, 6, %4\n"
"s_mov_b32 s72, %1\n"
"s_mov_b32 s73, %2\n"
"v_readfirstlane_b32 s68, v4\n"
"v_lshlrev_b32 v2, 4, v1\n"
"s_and_b32 s74, %3, 7\n"
"s_lshr_b32 s75, %3, 3\n"
"s_lshr_b32 s69, s75, 2\n"
"s_and_b32 s60, s75, 3\n"
"s_lshl_b32 s74, s74, 2\n"
"s_add_u32 s70, s74, s60\n"
"s_and_b32 s71, s68, 3\n"
"v_lshrrev_b32 v6, 3, v1\n"
"v_and_b32 v5, 7, v1\n"
"v_and_b32 v7, 31, v1\n"
"v_lshrrev_b32 v8, 5, v1\n"
"v_lshrrev_b32 v9, 4, v1\n"
"v_bfe_u32 v10, v1, 2, 2\n"
"v_and_b32 v11, 3, v1\n"
"s_bfe_u32 s81, s69, 65536\n"
"s_lshl_b32 s81, s81, 2\n"
"s_xor_b32 s81, s81, s68\n"
"s_lshl_b32 s74, s70, 6\n"
"s_lshl_b32 s75, s81, 3\n"
"s_add_u32 s74, s74, s75\n"
"s_lshl_b32 s94, s74, 10\n"
"v_lshlrev_b32 v4, 10, v6\n"
"v_lshl_add_u32 v4, v5, 4, v4\n"
"s_mov_b32 s34, 2097152\n"
"s_mov_b32 s35, 0x20000\n"
"s_waitcnt lgkmcnt(0)\n"
"s_mov_b32 s32, s8\n"
"s_and_b32 s33, s9, 0xffff\n"
"s_cmp_ge_u32 s68, 4\n"
"s_cbranch_scc1 Lcls_1_%=\n"
"buffer_load_dwordx4 v[64:67], v4, s[32:35], s94 offen offset:0\n"
"buffer_load_dwordx4 v[68:71], v4, s[32:35], s94 offen offset:128\n"
"buffer_load_dwordx4 v[72:75], v4, s[32:35], s94 offen offset:256\n"
"buffer_load_dwordx4 v[76:79], v4, s[32:35], s94 offen offset:384\n"
"buffer_load_dwordx4 v[80:83], v4, s[32:35], s94 offen offset:512\n"
"buffer_load_dwordx4 v[84:87], v4, s[32:35], s94 offen offset:640\n"
"buffer_load_dwordx4 v[88:91], v4, s[32:35], s94 offen offset:768\n"
"buffer_load_dwordx4 v[92:95], v4, s[32:35], s94 offen offset:896\n"
"Lcls_1_%=:\n"
"s_cmp_lg_u32 s68, 7\n"
"s_cbranch_scc1 Lncp_3_%=\n"
"s_getpc_b64 s[52:53]\n"
"Lhere_4_%=:\n"
"s_and_b32 s53, s53, 0xffff\n"
"s_mov_b32 s54, Lend_2_%=-Lhere_4_%=\n"
"s_mov_b32 s55, 0x20000\n"
"v_lshlrev_b32 v16, 6, v1\n"
"s_lshr_b32 s75, %3, 3\n"
"s_lshl_b32 s75, s75, 10\n"
"s_mov_b64 exec, 0xffff\n"
"buffer_load_dword v254, v16, s[52:55], s75 offen\n"
"s_mov_b64 exec, -1\n"
"Lncp_3_%=:\n"
"s_cmp_lg_u32 s69, 0\n"
"s_cbranch_scc1 Lnokw_5_%=\n"
"s_and_b32 s98, s68, 1\n"
"s_lshr_b32 s99, s68, 1\n"
"s_cmp_eq_u32 s98, 0\n"
"s_cselect_b32 s64, s12, s14\n"
"s_cselect_b32 s65, s13, s15\n"
"s_and_b32 s65, s65, 0xffff\n"
"s_mov_b32 s66, 16384\n"
"s_mov_b32 s67, 0x20000\n"
"s_lshl_b32 s87, s99, 13\n"
"s_lshl_b32 s76, s98, 14\n"
"s_add_u32 s76, s76, s87\n"
"s_add_u32 s96, s76, 65536\n"
"v_and_b32 v12, 1, v9\n"
"v_lshlrev_b32 v12, 7, v12\n"
"v_xor_b32 v222, v12, v2\n"
"v_and_b32 v12, 15, v1\n"
"s_and_b32 s74, s99, 1\n"
"s_lshr_b32 s75, s99, 1\n"
"s_lshl_b32 s76, s74, 4\n"
"v_add_u32 v13, s76, v12\n"
"v_and_b32 v17, 1, v9\n"
"v_lshrrev_b32 v18, 1, v9\n"
"v_lshlrev_b32 v19, 9, v17\n"
"v_lshl_add_u32 v19, v18, 10, v19\n"
"s_lshl_b32 s76, s75, 15\n"
"v_add_u32 v19, s76, v19\n"
"v_xor_b32 v16, v9, v13\n"
"v_lshl_add_u32 v216, v16, 4, v19\n"
"v_xor_b32 v16, 4, v16\n"
"v_lshl_add_u32 v16, v16, 4, v19\n"
"v_add_u32 v217, 2048, v16\n"
"v_lshl_add_u32 v16, v9, 3, v10\n"
"v_lshlrev_b32 v16, 5, v16\n"
"v_lshl_add_u32 v16, v11, 3, v16\n"
"s_lshl_b32 s76, s98, 14\n"
"s_add_u32 s76, s76, 65536\n"
"v_add_u32 v16, s76, v16\n"
"v_lshl_add_u32 v218, v17, 7, v16\n"
"v_xor_b32 v219, 128, v218\n"
"s_cmp_eq_u32 s98, 1\n"
"s_cbranch_scc1 Lkva_6_%=\n"
"s_lshl_b32 s77, s75, 7\n"
"s_add_u32 s77, s77, 138752\n"
"v_lshl_add_u32 v220, v13, 2, s77\n"
"v_lshl_add_u32 v16, v17, 5, v13\n"
"v_lshlrev_b32 v16, 4, v16\n"
"v_lshl_add_u32 v221, v18, 3, v16\n"
"s_branch Lkvb_7_%=\n"
"Lkva_6_%=:\n"
"s_lshl_b32 s77, s99, 6\n"
"s_add_u32 s77, s77, 138752\n"
"v_lshl_add_u32 v220, v9, 4, s77\n"
"v_lshl_add_u32 v16, v18, 4, v12\n"
"v_lshlrev_b32 v16, 4, v16\n"
"v_lshl_add_u32 v16, v17, 3, v16\n"
"s_lshl_b32 s77, s74, 9\n"
"s_add_u32 s77, s77, 1024\n"
"v_add_u32 v221, s77, v16\n"
"Lkvb_7_%=:\n"
"s_lshl_b32 s76, s70, 1\n"
"s_add_u32 s76, s76, s75\n"
"s_lshl_b32 s99, s76, 11\n"
"s_lshl_b32 s77, s70, 2\n"
"s_lshr_b32 s74, s68, 1\n"
"s_add_u32 s77, s77, s74\n"
"s_lshr_b32 s74, s77, 4\n"
"s_lshl_b32 s74, s74, 9\n"
"s_and_b32 s77, s77, 15\n"
"s_lshl_b32 s77, s77, 4\n"
"s_add_u32 s77, s77, s74\n"
"s_add_u32 s97, s77, 7901184\n"
"v_bfe_u32 v16, v1, 4, 1\n"
"v_lshrrev_b32 v17, 5, v1\n"
"v_lshlrev_b32 v16, 8, v16\n"
"v_lshl_add_u32 v223, v17, 3, v16\n"
"Lnokw_5_%=:\n"
"s_mov_b32 s40, s28\n"
"s_and_b32 s41, s29, 0xffff\n"
"s_mov_b32 s42, 16777216\n"
"s_mov_b32 s43, 0x20000\n"
"s_and_b32 s77, s81, 3\n"
"s_lshl_b32 s77, s77, 3\n"
"v_add_u32 v16, s77, v6\n"
"v_xor_b32 v16, v16, v5\n"
"v_lshlrev_b32 v16, 4, v16\n"
"v_lshl_add_u32 v16, v5, 9, v16\n"
"s_lshr_b32 s77, s81, 2\n"
"s_lshl_b32 s77, s77, 15\n"
"v_add_u32 v224, s77, v16\n"
"s_lshl_b32 s77, s81, 5\n"
"s_add_u32 s77, s77, 138752\n"
"v_lshl_add_u32 v225, v6, 2, s77\n"
"v_lshlrev_b32 v16, 2, v7\n"
"v_add_u32 v230, 138752, v16\n"
"s_cmp_ge_u32 s68, 4\n"
"s_cbranch_scc1 Lxb_8_%=\n"
"s_mul_i32 s75, s69, 6\n"
"s_add_u32 s75, s75, s71\n"
"s_sub_u32 s85, s75, 5\n"
"s_lshl_b32 s75, s69, 7\n"
"s_sub_u32 s76, s71, 2\n"
"s_lshl_b32 s76, s76, 6\n"
"s_add_u32 s75, s75, s76\n"
"s_lshl_b32 s76, s85, 6\n"
"s_cmp_ge_u32 s71, 2\n"
"s_cselect_b32 s61, s75, s76\n"
"s_movk_i32 s78, 2752\n"
"s_cselect_b32 s78, 1024, s78\n"
"s_cselect_b32 s85, 64, s85\n"
"s_cselect_b32 s36, s10, s18\n"
"s_cselect_b32 s37, s11, s19\n"
"s_mov_b32 s38, 1409024\n"
"s_cselect_b32 s38, 524288, s38\n"
"s_and_b32 s37, s37, 0xffff\n"
"s_mov_b32 s39, 0x20000\n"
"s_lshl_b32 s83, s71, 14\n"
"s_add_u32 s80, s83, 65536\n"
"v_lshrrev_b32 v226, 2, v1\n"
"v_mul_u32_u24 v226, s78, v226\n"
"v_lshl_add_u32 v226, v11, 4, v226\n"
"s_lshl_b32 s78, s78, 4\n"
"s_mul_i32 s75, s60, s78\n"
"s_lshl_b32 s75, s75, 3\n"
"s_add_u32 s79, s61, s75\n"
"s_lshl_b32 s75, s60, 13\n"
"v_lshl_add_u32 v18, v8, 9, s75\n"
"v_xor_b32 v19, v8, v7\n"
"v_lshl_add_u32 v232, v19, 4, v18\n"
"v_xor_b32 v20, 2, v19\n"
"v_lshl_add_u32 v20, v20, 4, v18\n"
"v_add_u32 v233, 1024, v20\n"
"v_xor_b32 v20, 4, v19\n"
"v_lshl_add_u32 v20, v20, 4, v18\n"
"v_add_u32 v234, 2048, v20\n"
"v_xor_b32 v20, 6, v19\n"
"v_lshl_add_u32 v20, v20, 4, v18\n"
"v_add_u32 v235, 3072, v20\n"
"v_lshl_add_u32 v18, v8, 3, v10\n"
"v_lshlrev_b32 v18, 6, v18\n"
"v_and_b32 v19, 1, v9\n"
"v_lshl_add_u32 v18, v19, 5, v18\n"
"v_lshl_add_u32 v18, v11, 3, v18\n"
"v_add_u32 v228, s80, v18\n"
"s_mov_b32 s56, s20\n"
"s_and_b32 s57, s21, 0xffff\n"
"s_mov_b32 s58, 2752\n"
"s_mov_b32 s59, 0x20000\n"
"s_lshl_b32 s75, s85, 5\n"
"v_add_lshl_u32 v227, v7, s75, 1\n"
"buffer_load_ushort v227, v227, s[56:59], 0 offen\n"
"s_mul_i32 s74, s70, 172\n"
"s_lshl_b32 s75, s85, 1\n"
"s_add_u32 s75, s74, s75\n"
"s_lshl_b32 s75, s75, 10\n"
"s_add_u32 s84, s75, 131072\n"
"s_lshl_b32 s75, s68, 2\n"
"v_add_lshl_u32 v231, v11, s75, 7\n"
"s_mov_b32 s82, 7864320\n"
"s_lshl_b32 s75, s70, 6\n"
"s_lshl_b32 s76, s69, 2\n"
"s_add_u32 s75, s75, s76\n"
"s_add_u32 s75, s75, s71\n"
"s_lshl_b32 s75, s75, 10\n"
"s_add_u32 s86, s75, 5767168\n"
"s_cmp_ge_u32 s71, 2\n"
"s_cselect_b32 s75, 1, s69\n"
"s_cmp_lg_u32 s75, 0\n"
"s_cselect_b64 vcc, -1, 0\n"
"s_waitcnt vmcnt(1)\n"
"s_branch Lxj_9_%=\n"
"Lxb_8_%=:\n"
"buffer_load_dwordx4 v[64:67], v4, s[32:35], s94 offen offset:0\n"
"buffer_load_dwordx4 v[68:71], v4, s[32:35], s94 offen offset:128\n"
"buffer_load_dwordx4 v[72:75], v4, s[32:35], s94 offen offset:256\n"
"buffer_load_dwordx4 v[76:79], v4, s[32:35], s94 offen offset:384\n"
"buffer_load_dwordx4 v[80:83], v4, s[32:35], s94 offen offset:512\n"
"buffer_load_dwordx4 v[84:87], v4, s[32:35], s94 offen offset:640\n"
"buffer_load_dwordx4 v[88:91], v4, s[32:35], s94 offen offset:768\n"
"buffer_load_dwordx4 v[92:95], v4, s[32:35], s94 offen offset:896\n"
"s_setprio 1\n"
"s_mov_b64 vcc, 0\n"
"s_waitcnt vmcnt(0)\n"
"Lxj_9_%=:\n"
"s_mov_b32 s74, 0x3f803f80\n"
"s_mov_b32 s75, 0x00003f80\n"
"s_mov_b32 s76, 0x3f800000\n"
"v_mov_b32 v16, 0\n"
"v_mov_b32 v17, 0\n"
"s_lshr_b32 s77, s68, 2\n"
"s_or_b32 s77, s77, s69\n"
"s_cbranch_scc1 Lnokw2_10_%=\n"
"s_mov_b32 m0, s96\n"
"s_add_u32 s77, s87, 4096\n"
"buffer_load_dwordx4 v222, s[64:67], s87 offen offset:0 lds\n"
"buffer_load_dwordx4 v222, s[64:67], s87 offen offset:1024 lds\n"
"buffer_load_dwordx4 v222, s[64:67], s87 offen offset:2048 lds\n"
"buffer_load_dwordx4 v222, s[64:67], s87 offen offset:3072 lds\n"
"s_add_u32 m0, s96, 4096\n"
"s_nop 0\n"
"buffer_load_dwordx4 v222, s[64:67], s77 offen offset:0 lds\n"
"buffer_load_dwordx4 v222, s[64:67], s77 offen offset:1024 lds\n"
"buffer_load_dwordx4 v222, s[64:67], s77 offen offset:2048 lds\n"
"buffer_load_dwordx4 v222, s[64:67], s77 offen offset:3072 lds\n"
"Lnokw2_10_%=:\n"
"v_dot2c_f32_bf16 v16, s74, v64\n"
"v_dot2c_f32_bf16 v17, v64, v64\n"
"v_dot2c_f32_bf16 v16, s74, v65\n"
"v_dot2c_f32_bf16 v17, v65, v65\n"
"v_dot2c_f32_bf16 v16, s74, v66\n"
"v_dot2c_f32_bf16 v17, v66, v66\n"
"v_dot2c_f32_bf16 v16, s74, v67\n"
"v_dot2c_f32_bf16 v17, v67, v67\n"
"v_dot2c_f32_bf16 v16, s74, v68\n"
"v_dot2c_f32_bf16 v17, v68, v68\n"
"v_dot2c_f32_bf16 v16, s74, v69\n"
"v_dot2c_f32_bf16 v17, v69, v69\n"
"v_dot2c_f32_bf16 v16, s74, v70\n"
"v_dot2c_f32_bf16 v17, v70, v70\n"
"v_dot2c_f32_bf16 v16, s74, v71\n"
"v_dot2c_f32_bf16 v17, v71, v71\n"
"v_dot2c_f32_bf16 v16, s74, v72\n"
"v_dot2c_f32_bf16 v17, v72, v72\n"
"v_dot2c_f32_bf16 v16, s74, v73\n"
"v_dot2c_f32_bf16 v17, v73, v73\n"
"v_dot2c_f32_bf16 v16, s74, v74\n"
"v_dot2c_f32_bf16 v17, v74, v74\n"
"v_dot2c_f32_bf16 v16, s74, v75\n"
"v_dot2c_f32_bf16 v17, v75, v75\n"
"v_dot2c_f32_bf16 v16, s74, v76\n"
"v_dot2c_f32_bf16 v17, v76, v76\n"
"v_dot2c_f32_bf16 v16, s74, v77\n"
"v_dot2c_f32_bf16 v17, v77, v77\n"
"v_dot2c_f32_bf16 v16, s74, v78\n"
"v_dot2c_f32_bf16 v17, v78, v78\n"
"v_dot2c_f32_bf16 v16, s74, v79\n"
"v_dot2c_f32_bf16 v17, v79, v79\n"
"v_dot2c_f32_bf16 v16, s74, v80\n"
"v_dot2c_f32_bf16 v17, v80, v80\n"
"v_dot2c_f32_bf16 v16, s74, v81\n"
"v_dot2c_f32_bf16 v17, v81, v81\n"
"v_dot2c_f32_bf16 v16, s74, v82\n"
"v_dot2c_f32_bf16 v17, v82, v82\n"
"v_dot2c_f32_bf16 v16, s74, v83\n"
"v_dot2c_f32_bf16 v17, v83, v83\n"
"v_dot2c_f32_bf16 v16, s74, v84\n"
"v_dot2c_f32_bf16 v17, v84, v84\n"
"v_dot2c_f32_bf16 v16, s74, v85\n"
"v_dot2c_f32_bf16 v17, v85, v85\n"
"v_dot2c_f32_bf16 v16, s74, v86\n"
"v_dot2c_f32_bf16 v17, v86, v86\n"
"v_dot2c_f32_bf16 v16, s74, v87\n"
"v_dot2c_f32_bf16 v17, v87, v87\n"
"v_dot2c_f32_bf16 v16, s74, v88\n"
"v_dot2c_f32_bf16 v17, v88, v88\n"
"v_dot2c_f32_bf16 v16, s74, v89\n"
"v_dot2c_f32_bf16 v17, v89, v89\n"
"v_dot2c_f32_bf16 v16, s74, v90\n"
"v_dot2c_f32_bf16 v17, v90, v90\n"
"v_dot2c_f32_bf16 v16, s74, v91\n"
"v_dot2c_f32_bf16 v17, v91, v91\n"
"v_dot2c_f32_bf16 v16, s74, v92\n"
"v_dot2c_f32_bf16 v17, v92, v92\n"
"v_dot2c_f32_bf16 v16, s74, v93\n"
"v_dot2c_f32_bf16 v17, v93, v93\n"
"v_dot2c_f32_bf16 v16, s74, v94\n"
"v_dot2c_f32_bf16 v17, v94, v94\n"
"v_dot2c_f32_bf16 v16, s74, v95\n"
"v_dot2c_f32_bf16 v17, v95, v95\n"
"s_nop 1\n"
"v_add_f32_dpp v16, v16, v16 quad_perm:[1,0,3,2] row_mask:0xf bank_mask:0xf\n"
"v_add_f32_dpp v17, v17, v17 quad_perm:[1,0,3,2] row_mask:0xf bank_mask:0xf\n"
"s_nop 0\n"
"v_add_f32_dpp v16, v16, v16 quad_perm:[2,3,0,1] row_mask:0xf bank_mask:0xf\n"
"v_add_f32_dpp v17, v17, v17 quad_perm:[2,3,0,1] row_mask:0xf bank_mask:0xf\n"
"s_nop 0\n"
"v_add_f32_dpp v16, v16, v16 row_half_mirror row_mask:0xf bank_mask:0xf\n"
"v_add_f32_dpp v17, v17, v17 row_half_mirror row_mask:0xf bank_mask:0xf\n"
"v_mul_f32 v18, 0x3b000000, v16\n"
"v_mul_f32 v19, 0x3b000000, v17\n"
"v_cvt_pk_bf16_f32 v3, -v18, -v18\n"
"v_fma_f32 v19, -v18, v18, v19\n"
"v_lshlrev_b32 v3, 16, v3\n"
"v_max_f32 v19, 0, v19\n"
"v_cvt_pk_bf16_f32 v19, v19, v19\n"
"v_lshlrev_b32 v19, 16, v19\n"
"v_add_f32 v19, 0x3727c5ac, v19\n"
"v_cvt_pk_bf16_f32 v19, v19, v19\n"
"v_lshlrev_b32 v19, 16, v19\n"
"v_sqrt_f32 v19, v19\n"
"v_dot2_f32_bf16 v24, v64, s75, v3\n"
"v_dot2_f32_bf16 v25, v64, s76, v3\n"
"v_dot2_f32_bf16 v26, v65, s75, v3\n"
"v_dot2_f32_bf16 v27, v65, s76, v3\n"
"v_dot2_f32_bf16 v28, v66, s75, v3\n"
"v_dot2_f32_bf16 v29, v66, s76, v3\n"
"v_dot2_f32_bf16 v30, v67, s75, v3\n"
"v_dot2_f32_bf16 v31, v67, s76, v3\n"
"v_cvt_pk_bf16_f32 v19, v19, v19\n"
"v_lshlrev_b32 v19, 16, v19\n"
"ds_write_b32 v225, v19\n"
"s_cbranch_vccz Lnw_11_%=\n"
"s_add_u32 m0, s80, 0\n"
"s_nop 0\n"
"buffer_load_dwordx4 v226, s[36:39], s79 offen lds\n"
"s_add_u32 s79, s79, s78\n"
"Lnw_11_%=:\n"
"v_cvt_pk_bf16_f32 v32, v24, v25\n"
"v_cvt_pk_bf16_f32 v33, v26, v27\n"
"v_cvt_pk_bf16_f32 v34, v28, v29\n"
"v_cvt_pk_bf16_f32 v35, v30, v31\n"
"v_dot2_f32_bf16 v36, v68, s75, v3\n"
"v_dot2_f32_bf16 v37, v68, s76, v3\n"
"v_dot2_f32_bf16 v38, v69, s75, v3\n"
"v_dot2_f32_bf16 v39, v69, s76, v3\n"
"v_dot2_f32_bf16 v40, v70, s75, v3\n"
"v_dot2_f32_bf16 v41, v70, s76, v3\n"
"v_dot2_f32_bf16 v42, v71, s75, v3\n"
"v_dot2_f32_bf16 v43, v71, s76, v3\n"
"ds_write_b128 v224, v[32:35] offset:0\n"
"s_cbranch_vccz Lnw_12_%=\n"
"s_add_u32 m0, s80, 1024\n"
"s_nop 0\n"
"buffer_load_dwordx4 v226, s[36:39], s79 offen lds\n"
"s_add_u32 s79, s79, s78\n"
"Lnw_12_%=:\n"
"v_cvt_pk_bf16_f32 v44, v36, v37\n"
"v_cvt_pk_bf16_f32 v45, v38, v39\n"
"v_cvt_pk_bf16_f32 v46, v40, v41\n"
"v_cvt_pk_bf16_f32 v47, v42, v43\n"
"v_dot2_f32_bf16 v24, v72, s75, v3\n"
"v_dot2_f32_bf16 v25, v72, s76, v3\n"
"v_dot2_f32_bf16 v26, v73, s75, v3\n"
"v_dot2_f32_bf16 v27, v73, s76, v3\n"
"v_dot2_f32_bf16 v28, v74, s75, v3\n"
"v_dot2_f32_bf16 v29, v74, s76, v3\n"
"v_dot2_f32_bf16 v30, v75, s75, v3\n"
"v_dot2_f32_bf16 v31, v75, s76, v3\n"
"ds_write_b128 v224, v[44:47] offset:4096\n"
"s_cbranch_vccz Lnw_13_%=\n"
"s_add_u32 m0, s80, 2048\n"
"s_nop 0\n"
"buffer_load_dwordx4 v226, s[36:39], s79 offen lds\n"
"s_add_u32 s79, s79, s78\n"
"Lnw_13_%=:\n"
"v_cvt_pk_bf16_f32 v32, v24, v25\n"
"v_cvt_pk_bf16_f32 v33, v26, v27\n"
"v_cvt_pk_bf16_f32 v34, v28, v29\n"
"v_cvt_pk_bf16_f32 v35, v30, v31\n"
"v_dot2_f32_bf16 v36, v76, s75, v3\n"
"v_dot2_f32_bf16 v37, v76, s76, v3\n"
"v_dot2_f32_bf16 v38, v77, s75, v3\n"
"v_dot2_f32_bf16 v39, v77, s76, v3\n"
"v_dot2_f32_bf16 v40, v78, s75, v3\n"
"v_dot2_f32_bf16 v41, v78, s76, v3\n"
"v_dot2_f32_bf16 v42, v79, s75, v3\n"
"v_dot2_f32_bf16 v43, v79, s76, v3\n"
"ds_write_b128 v224, v[32:35] offset:8192\n"
"s_cbranch_vccz Lnw_14_%=\n"
"s_add_u32 m0, s80, 3072\n"
"s_nop 0\n"
"buffer_load_dwordx4 v226, s[36:39], s79 offen lds\n"
"s_add_u32 s79, s79, s78\n"
"Lnw_14_%=:\n"
"v_cvt_pk_bf16_f32 v44, v36, v37\n"
"v_cvt_pk_bf16_f32 v45, v38, v39\n"
"v_cvt_pk_bf16_f32 v46, v40, v41\n"
"v_cvt_pk_bf16_f32 v47, v42, v43\n"
"v_dot2_f32_bf16 v24, v80, s75, v3\n"
"v_dot2_f32_bf16 v25, v80, s76, v3\n"
"v_dot2_f32_bf16 v26, v81, s75, v3\n"
"v_dot2_f32_bf16 v27, v81, s76, v3\n"
"v_dot2_f32_bf16 v28, v82, s75, v3\n"
"v_dot2_f32_bf16 v29, v82, s76, v3\n"
"v_dot2_f32_bf16 v30, v83, s75, v3\n"
"v_dot2_f32_bf16 v31, v83, s76, v3\n"
"ds_write_b128 v224, v[44:47] offset:12288\n"
"s_cbranch_vccz Lnw_15_%=\n"
"s_add_u32 m0, s80, 4096\n"
"s_nop 0\n"
"buffer_load_dwordx4 v226, s[36:39], s79 offen lds\n"
"s_add_u32 s79, s79, s78\n"
"Lnw_15_%=:\n"
"v_cvt_pk_bf16_f32 v32, v24, v25\n"
"v_cvt_pk_bf16_f32 v33, v26, v27\n"
"v_cvt_pk_bf16_f32 v34, v28, v29\n"
"v_cvt_pk_bf16_f32 v35, v30, v31\n"
"v_dot2_f32_bf16 v36, v84, s75, v3\n"
"v_dot2_f32_bf16 v37, v84, s76, v3\n"
"v_dot2_f32_bf16 v38, v85, s75, v3\n"
"v_dot2_f32_bf16 v39, v85, s76, v3\n"
"v_dot2_f32_bf16 v40, v86, s75, v3\n"
"v_dot2_f32_bf16 v41, v86, s76, v3\n"
"v_dot2_f32_bf16 v42, v87, s75, v3\n"
"v_dot2_f32_bf16 v43, v87, s76, v3\n"
"ds_write_b128 v224, v[32:35] offset:16384\n"
"s_cbranch_vccz Lnw_16_%=\n"
"s_add_u32 m0, s80, 5120\n"
"s_nop 0\n"
"buffer_load_dwordx4 v226, s[36:39], s79 offen lds\n"
"s_add_u32 s79, s79, s78\n"
"Lnw_16_%=:\n"
"v_cvt_pk_bf16_f32 v44, v36, v37\n"
"v_cvt_pk_bf16_f32 v45, v38, v39\n"
"v_cvt_pk_bf16_f32 v46, v40, v41\n"
"v_cvt_pk_bf16_f32 v47, v42, v43\n"
"v_dot2_f32_bf16 v24, v88, s75, v3\n"
"v_dot2_f32_bf16 v25, v88, s76, v3\n"
"v_dot2_f32_bf16 v26, v89, s75, v3\n"
"v_dot2_f32_bf16 v27, v89, s76, v3\n"
"v_dot2_f32_bf16 v28, v90, s75, v3\n"
"v_dot2_f32_bf16 v29, v90, s76, v3\n"
"v_dot2_f32_bf16 v30, v91, s75, v3\n"
"v_dot2_f32_bf16 v31, v91, s76, v3\n"
"ds_write_b128 v224, v[44:47] offset:20480\n"
"s_cbranch_vccz Lnw_17_%=\n"
"s_add_u32 m0, s80, 6144\n"
"s_nop 0\n"
"buffer_load_dwordx4 v226, s[36:39], s79 offen lds\n"
"s_add_u32 s79, s79, s78\n"
"Lnw_17_%=:\n"
"v_cvt_pk_bf16_f32 v32, v24, v25\n"
"v_cvt_pk_bf16_f32 v33, v26, v27\n"
"v_cvt_pk_bf16_f32 v34, v28, v29\n"
"v_cvt_pk_bf16_f32 v35, v30, v31\n"
"v_dot2_f32_bf16 v36, v92, s75, v3\n"
"v_dot2_f32_bf16 v37, v92, s76, v3\n"
"v_dot2_f32_bf16 v38, v93, s75, v3\n"
"v_dot2_f32_bf16 v39, v93, s76, v3\n"
"v_dot2_f32_bf16 v40, v94, s75, v3\n"
"v_dot2_f32_bf16 v41, v94, s76, v3\n"
"v_dot2_f32_bf16 v42, v95, s75, v3\n"
"v_dot2_f32_bf16 v43, v95, s76, v3\n"
"ds_write_b128 v224, v[32:35] offset:24576\n"
"s_cbranch_vccz Lnw_18_%=\n"
"s_add_u32 m0, s80, 7168\n"
"s_nop 0\n"
"buffer_load_dwordx4 v226, s[36:39], s79 offen lds\n"
"s_add_u32 s79, s79, s78\n"
"s_cmp_eq_u32 s60, 3\n"
"s_cselect_b32 s79, s61, s79\n"
"Lnw_18_%=:\n"
"v_cvt_pk_bf16_f32 v44, v36, v37\n"
"v_cvt_pk_bf16_f32 v45, v38, v39\n"
"v_cvt_pk_bf16_f32 v46, v40, v41\n"
"v_cvt_pk_bf16_f32 v47, v42, v43\n"
"ds_write_b128 v224, v[44:47] offset:28672\n"
"s_cbranch_vccz Lnwa_19_%=\n"
"Lnwa_19_%=:\n"
"s_cmp_lg_u32 s69, 0\n"
"s_cbranch_scc1 Lkww_20_%=\n"
"s_cbranch_vccnz Lkww2_21_%=\n"
"s_waitcnt vmcnt(0)\n"
"s_branch Lkww_20_%=\n"
"Lkww2_21_%=:\n"
"s_waitcnt vmcnt(8)\n"
"Lkww_20_%=:\n"
"s_waitcnt lgkmcnt(0)\n"
"s_barrier\n"
"ds_read_b32 v14, v230\n"
"ds_read_b32 v15, v230 offset:128\n"
"s_cmp_lg_u32 s69, 0\n"
"s_cbranch_scc1 Ljn_23_%=\n"
"s_cmp_eq_u32 s98, 1\n"
"s_cbranch_scc1 LkvV_24_%=\n"
"ds_read_b32 v56, v220\n"
"ds_read_b128 v[64:67], v216 offset:0\n"
"ds_read_b64_tr_b16 v[168:169], v218 offset:0\n"
"ds_read_b64_tr_b16 v[170:171], v219 offset:0\n"
"ds_read_b128 v[68:71], v217 offset:0\n"
"ds_read_b64_tr_b16 v[172:173], v218 offset:1024\n"
"ds_read_b64_tr_b16 v[174:175], v219 offset:1024\n"
"ds_read_b128 v[72:75], v216 offset:4096\n"
"ds_read_b64_tr_b16 v[176:177], v218 offset:2048\n"
"ds_read_b64_tr_b16 v[178:179], v219 offset:2048\n"
"ds_read_b128 v[76:79], v217 offset:4096\n"
"ds_read_b64_tr_b16 v[180:181], v218 offset:3072\n"
"ds_read_b64_tr_b16 v[182:183], v219 offset:3072\n"
"s_waitcnt lgkmcnt(9)\n"
"v_mfma_f32_16x16x32_bf16 v[60:63], v[168:171], v[64:67], 0\n"
"ds_read_b128 v[80:83], v216 offset:8192\n"
"ds_read_b64_tr_b16 v[184:185], v218 offset:4096\n"
"ds_read_b64_tr_b16 v[186:187], v219 offset:4096\n"
"v_rcp_f32 v56, v56\n"
"s_waitcnt lgkmcnt(9)\n"
"v_mfma_f32_16x16x32_bf16 v[60:63], v[172:175], v[68:71], v[60:63]\n"
"ds_read_b128 v[84:87], v217 offset:8192\n"
"ds_read_b64_tr_b16 v[188:189], v218 offset:5120\n"
"ds_read_b64_tr_b16 v[190:191], v219 offset:5120\n"
"s_waitcnt lgkmcnt(9)\n"
"v_mfma_f32_16x16x32_bf16 v[60:63], v[176:179], v[72:75], v[60:63]\n"
"ds_read_b128 v[88:91], v216 offset:12288\n"
"ds_read_b64_tr_b16 v[192:193], v218 offset:6144\n"
"ds_read_b64_tr_b16 v[194:195], v219 offset:6144\n"
"s_waitcnt lgkmcnt(9)\n"
"v_mfma_f32_16x16x32_bf16 v[60:63], v[180:183], v[76:79], v[60:63]\n"
"ds_read_b128 v[92:95], v217 offset:12288\n"
"ds_read_b64_tr_b16 v[196:197], v218 offset:7168\n"
"ds_read_b64_tr_b16 v[198:199], v219 offset:7168\n"
"s_waitcnt lgkmcnt(9)\n"
"v_mfma_f32_16x16x32_bf16 v[60:63], v[184:187], v[80:83], v[60:63]\n"
"ds_read_b128 v[64:67], v216 offset:16384\n"
"ds_read_b64_tr_b16 v[168:169], v218 offset:8192\n"
"ds_read_b64_tr_b16 v[170:171], v219 offset:8192\n"
"s_waitcnt lgkmcnt(9)\n"
"v_mfma_f32_16x16x32_bf16 v[60:63], v[188:191], v[84:87], v[60:63]\n"
"ds_read_b128 v[68:71], v217 offset:16384\n"
"ds_read_b64_tr_b16 v[172:173], v218 offset:9216\n"
"ds_read_b64_tr_b16 v[174:175], v219 offset:9216\n"
"s_waitcnt lgkmcnt(9)\n"
"v_mfma_f32_16x16x32_bf16 v[60:63], v[192:195], v[88:91], v[60:63]\n"
"ds_read_b128 v[72:75], v216 offset:20480\n"
"ds_read_b64_tr_b16 v[176:177], v218 offset:10240\n"
"ds_read_b64_tr_b16 v[178:179], v219 offset:10240\n"
"s_waitcnt lgkmcnt(9)\n"
"v_mfma_f32_16x16x32_bf16 v[60:63], v[196:199], v[92:95], v[60:63]\n"
"ds_read_b128 v[76:79], v217 offset:20480\n"
"ds_read_b64_tr_b16 v[180:181], v218 offset:11264\n"
"ds_read_b64_tr_b16 v[182:183], v219 offset:11264\n"
"s_waitcnt lgkmcnt(9)\n"
"v_mfma_f32_16x16x32_bf16 v[60:63], v[168:171], v[64:67], v[60:63]\n"
"ds_read_b128 v[80:83], v216 offset:24576\n"
"ds_read_b64_tr_b16 v[184:185], v218 offset:12288\n"
"ds_read_b64_tr_b16 v[186:187], v219 offset:12288\n"
"s_waitcnt lgkmcnt(9)\n"
"v_mfma_f32_16x16x32_bf16 v[60:63], v[172:175], v[68:71], v[60:63]\n"
"ds_read_b128 v[84:87], v217 offset:24576\n"
"ds_read_b64_tr_b16 v[188:189], v218 offset:13312\n"
"ds_read_b64_tr_b16 v[190:191], v219 offset:13312\n"
"v_mov_b32 v52, s72\n"
"v_mov_b32 v53, s73\n"
"v_mov_b32 v54, 0\n"
"s_lshl_b32 s98, s70, 7\n"
"s_add_u32 s98, s98, 7864320\n"
"s_waitcnt lgkmcnt(9)\n"
"v_mfma_f32_16x16x32_bf16 v[60:63], v[176:179], v[72:75], v[60:63]\n"
"ds_read_b128 v[88:91], v216 offset:28672\n"
"ds_read_b64_tr_b16 v[192:193], v218 offset:14336\n"
"ds_read_b64_tr_b16 v[194:195], v219 offset:14336\n"
"s_waitcnt lgkmcnt(9)\n"
"v_mfma_f32_16x16x32_bf16 v[44:47], v[180:183], v[76:79], 0\n"
"ds_read_b128 v[92:95], v217 offset:28672\n"
"ds_read_b64_tr_b16 v[196:197], v218 offset:15360\n"
"ds_read_b64_tr_b16 v[198:199], v219 offset:15360\n"
"s_waitcnt lgkmcnt(9)\n"
"v_mfma_f32_16x16x32_bf16 v[44:47], v[184:187], v[80:83], v[44:47]\n"
"v_mul_f32 v49, v60, v60\n"
"v_fmac_f32 v49, v61, v61\n"
"v_fmac_f32 v49, v62, v62\n"
"v_fmac_f32 v49, v63, v63\n"
"v_mul_f32 v49, v49, v56\n"
"v_mul_f32 v49, v49, v56\n"
"s_waitcnt lgkmcnt(6)\n"
"v_mfma_f32_16x16x32_bf16 v[44:47], v[188:191], v[84:87], v[44:47]\n"
"v_mov_b32 v50, v49\n"
"s_nop 1\n"
"v_permlane16_swap_b32 v49, v50\n"
"v_add_f32 v49, v49, v50\n"
"v_mov_b32 v50, v49\n"
"s_nop 1\n"
"v_permlane32_swap_b32 v49, v50\n"
"v_add_f32 v49, v49, v50\n"
"s_waitcnt lgkmcnt(3)\n"
"v_mfma_f32_16x16x32_bf16 v[44:47], v[192:195], v[88:91], v[44:47]\n"
"v_mov_b32 v50, v49\n"
"s_nop 1\n"
"v_max_f32_dpp v50, v50, v50 quad_perm:[1,0,3,2] row_mask:0xf bank_mask:0xf\n"
"s_nop 1\n"
"v_max_f32_dpp v50, v50, v50 quad_perm:[2,3,0,1] row_mask:0xf bank_mask:0xf\n"
"s_nop 1\n"
"v_max_f32_dpp v50, v50, v50 row_half_mirror row_mask:0xf bank_mask:0xf\n"
"s_nop 1\n"
"v_max_f32_dpp v50, v50, v50 row_mirror row_mask:0xf bank_mask:0xf\n"
"s_waitcnt lgkmcnt(0)\n"
"v_mfma_f32_16x16x32_bf16 v[44:47], v[196:199], v[92:95], v[44:47]\n"
"v_cmp_eq_f32 vcc, v49, v50\n"
"s_nop 0\n"
"s_and_b32 s74, vcc_lo, 0xffff\n"
"s_ff1_i32_b32 s74, s74\n"
"s_max_i32 s74, s74, 0\n"
"s_lshl_b32 s74, 0x10001, s74\n"
"s_nop 1\n"
"v_add_f32 v60, v60, v44\n"
"v_add_f32 v61, v61, v45\n"
"v_add_f32 v62, v62, v46\n"
"v_add_f32 v63, v63, v47\n"
"v_mul_f32 v60, v56, v60\n"
"v_mul_f32 v61, v56, v61\n"
"v_mul_f32 v62, v56, v62\n"
"v_mul_f32 v63, v56, v63\n"
"v_cvt_pk_bf16_f32 v58, v60, v61\n"
"v_cvt_pk_bf16_f32 v59, v62, v63\n"
"buffer_store_dwordx2 v[58:59], v221, s[40:43], s99 offen sc1\n"
"s_mov_b32 exec_lo, s74\n"
"s_mov_b32 exec_hi, s74\n"
"buffer_store_dwordx2 v[58:59], v223, s[40:43], s97 offen sc1\n"
"s_mov_b64 exec, -1\n"
"s_cmp_ge_u32 s71, 2\n"
"s_cselect_b64 vcc, -1, 0\n"
"s_branch Lkvs2_26_%=\n"
"LkvV_24_%=:\n"
"ds_read_b128 v[56:59], v220\n"
"ds_read_b128 v[64:67], v216 offset:0\n"
"ds_read_b64_tr_b16 v[168:169], v218 offset:0\n"
"ds_read_b64_tr_b16 v[170:171], v219 offset:0\n"
"ds_read_b128 v[68:71], v217 offset:0\n"
"ds_read_b64_tr_b16 v[172:173], v218 offset:1024\n"
"ds_read_b64_tr_b16 v[174:175], v219 offset:1024\n"
"ds_read_b128 v[72:75], v216 offset:4096\n"
"ds_read_b64_tr_b16 v[176:177], v218 offset:2048\n"
"ds_read_b64_tr_b16 v[178:179], v219 offset:2048\n"
"ds_read_b128 v[76:79], v217 offset:4096\n"
"ds_read_b64_tr_b16 v[180:181], v218 offset:3072\n"
"ds_read_b64_tr_b16 v[182:183], v219 offset:3072\n"
"s_waitcnt lgkmcnt(9)\n"
"v_mfma_f32_16x16x32_bf16 v[60:63], v[64:67], v[168:171], 0\n"
"ds_read_b128 v[80:83], v216 offset:8192\n"
"ds_read_b64_tr_b16 v[184:185], v218 offset:4096\n"
"ds_read_b64_tr_b16 v[186:187], v219 offset:4096\n"
"v_rcp_f32 v56, v56\n"
"v_rcp_f32 v57, v57\n"
"v_rcp_f32 v58, v58\n"
"v_rcp_f32 v59, v59\n"
"s_waitcnt lgkmcnt(9)\n"
"v_mfma_f32_16x16x32_bf16 v[60:63], v[68:71], v[172:175], v[60:63]\n"
"ds_read_b128 v[84:87], v217 offset:8192\n"
"ds_read_b64_tr_b16 v[188:189], v218 offset:5120\n"
"ds_read_b64_tr_b16 v[190:191], v219 offset:5120\n"
"s_waitcnt lgkmcnt(9)\n"
"v_mfma_f32_16x16x32_bf16 v[60:63], v[72:75], v[176:179], v[60:63]\n"
"ds_read_b128 v[88:91], v216 offset:12288\n"
"ds_read_b64_tr_b16 v[192:193], v218 offset:6144\n"
"ds_read_b64_tr_b16 v[194:195], v219 offset:6144\n"
"s_waitcnt lgkmcnt(9)\n"
"v_mfma_f32_16x16x32_bf16 v[60:63], v[76:79], v[180:183], v[60:63]\n"
"ds_read_b128 v[92:95], v217 offset:12288\n"
"ds_read_b64_tr_b16 v[196:197], v218 offset:7168\n"
"ds_read_b64_tr_b16 v[198:199], v219 offset:7168\n"
"s_waitcnt lgkmcnt(9)\n"
"v_mfma_f32_16x16x32_bf16 v[60:63], v[80:83], v[184:187], v[60:63]\n"
"ds_read_b128 v[64:67], v216 offset:16384\n"
"ds_read_b64_tr_b16 v[168:169], v218 offset:8192\n"
"ds_read_b64_tr_b16 v[170:171], v219 offset:8192\n"
"s_waitcnt lgkmcnt(9)\n"
"v_mfma_f32_16x16x32_bf16 v[60:63], v[84:87], v[188:191], v[60:63]\n"
"ds_read_b128 v[68:71], v217 offset:16384\n"
"ds_read_b64_tr_b16 v[172:173], v218 offset:9216\n"
"ds_read_b64_tr_b16 v[174:175], v219 offset:9216\n"
"s_waitcnt lgkmcnt(9)\n"
"v_mfma_f32_16x16x32_bf16 v[60:63], v[88:91], v[192:195], v[60:63]\n"
"ds_read_b128 v[72:75], v216 offset:20480\n"
"ds_read_b64_tr_b16 v[176:177], v218 offset:10240\n"
"ds_read_b64_tr_b16 v[178:179], v219 offset:10240\n"
"s_waitcnt lgkmcnt(9)\n"
"v_mfma_f32_16x16x32_bf16 v[60:63], v[92:95], v[196:199], v[60:63]\n"
"ds_read_b128 v[76:79], v217 offset:20480\n"
"ds_read_b64_tr_b16 v[180:181], v218 offset:11264\n"
"ds_read_b64_tr_b16 v[182:183], v219 offset:11264\n"
"s_waitcnt lgkmcnt(9)\n"
"v_mfma_f32_16x16x32_bf16 v[60:63], v[64:67], v[168:171], v[60:63]\n"
"ds_read_b128 v[80:83], v216 offset:24576\n"
"ds_read_b64_tr_b16 v[184:185], v218 offset:12288\n"
"ds_read_b64_tr_b16 v[186:187], v219 offset:12288\n"
"s_waitcnt lgkmcnt(9)\n"
"v_mfma_f32_16x16x32_bf16 v[60:63], v[68:71], v[172:175], v[60:63]\n"
"ds_read_b128 v[84:87], v217 offset:24576\n"
"ds_read_b64_tr_b16 v[188:189], v218 offset:13312\n"
"ds_read_b64_tr_b16 v[190:191], v219 offset:13312\n"
"v_mov_b32 v52, s72\n"
"v_mov_b32 v53, s73\n"
"v_mov_b32 v54, 0\n"
"s_lshl_b32 s98, s70, 7\n"
"s_add_u32 s98, s98, 7864320\n"
"s_waitcnt lgkmcnt(9)\n"
"v_mfma_f32_16x16x32_bf16 v[60:63], v[72:75], v[176:179], v[60:63]\n"
"ds_read_b128 v[88:91], v216 offset:28672\n"
"ds_read_b64_tr_b16 v[192:193], v218 offset:14336\n"
"ds_read_b64_tr_b16 v[194:195], v219 offset:14336\n"
"s_waitcnt lgkmcnt(9)\n"
"v_mfma_f32_16x16x32_bf16 v[60:63], v[76:79], v[180:183], v[60:63]\n"
"ds_read_b128 v[92:95], v217 offset:28672\n"
"ds_read_b64_tr_b16 v[196:197], v218 offset:15360\n"
"ds_read_b64_tr_b16 v[198:199], v219 offset:15360\n"
"s_waitcnt lgkmcnt(9)\n"
"v_mfma_f32_16x16x32_bf16 v[60:63], v[80:83], v[184:187], v[60:63]\n"
"s_waitcnt lgkmcnt(6)\n"
"v_mfma_f32_16x16x32_bf16 v[60:63], v[84:87], v[188:191], v[60:63]\n"
"s_waitcnt lgkmcnt(3)\n"
"v_mfma_f32_16x16x32_bf16 v[60:63], v[88:91], v[192:195], v[60:63]\n"
"s_waitcnt lgkmcnt(0)\n"
"v_mfma_f32_16x16x32_bf16 v[60:63], v[92:95], v[196:199], v[60:63]\n"
"s_nop 7\n"
"v_mul_f32 v60, v56, v60\n"
"v_mul_f32 v61, v57, v61\n"
"v_mul_f32 v62, v58, v62\n"
"v_mul_f32 v63, v59, v63\n"
"Lkvst_25_%=:\n"
"v_cvt_pk_bf16_f32 v60, v60, v61\n"
"v_cvt_pk_bf16_f32 v61, v62, v63\n"
"buffer_store_dwordx2 v[60:61], v221, s[40:43], s99 offen sc1\n"
"Lkvs2_26_%=:\n"
"s_cmp_ge_u32 s68, 4\n"
"s_cbranch_scc1 Lcls_27_%=\n"
"v_and_b32 v4, 31, v1\n"
"v_lshrrev_b32 v5, 5, v1\n"
"v_add_u32 v236, 65536, v2\n"
"v_add_u32 v237, 98304, v2\n"
"v_lshlrev_b32 v5, 8, v5\n"
"v_lshl_add_u32 v5, v4, 4, v5\n"
"v_add_u32 v238, 66560, v5\n"
"v_add_u32 v239, 99328, v5\n"
"s_lshr_b32 s75, s71, 1\n"
"s_lshl_b32 s75, s75, 14\n"
"s_and_b32 s76, s71, 1\n"
"s_lshl_b32 s76, s76, 10\n"
"s_add_u32 s75, s75, s76\n"
"s_add_u32 s75, s75, 110592\n"
"v_add_u32 v240, s75, v2\n"
"s_lshl_b32 s75, s71, 13\n"
"s_add_u32 s75, s75, 131072\n"
"v_add_u32 v242, s75, v2\n"
"v_lshl_add_u32 v5, v1, 2, s75\n"
"v_add_u32 v241, 7680, v5\n"
"v_add_u32 v229, 4096, v5\n"
"v_bfe_u32 v5, v1, 4, 1\n"
"v_lshlrev_b32 v225, 13, v5\n"
"v_lshrrev_b32 v5, 5, v1\n"
"v_lshl_add_u32 v225, v5, 8, v225\n"
"v_and_b32 v5, 15, v1\n"
"v_lshl_add_u32 v225, v5, 4, v225\n"
"v_add_u32 v225, 138240, v225\n"
"v_lshrrev_b32 v5, 3, v1\n"
"s_lshl_b32 s75, s70, 3\n"
"v_add_lshl_u32 v246, v5, s75, 7\n"
"v_and_b32 v5, 7, v1\n"
"v_lshl_add_u32 v246, v5, 3, v246\n"
"v_mov_b32 v204, s72\n"
"v_mov_b32 v205, s73\n"
"v_mov_b32 v206, 0\n"
"s_lshl_b32 s76, s70, 3\n"
"s_add_u32 s76, s76, s69\n"
"s_lshl_b32 s76, s76, 4\n"
"s_add_u32 s76, s76, s68\n"
"s_lshl_b32 s76, s76, 3\n"
"s_add_u32 s85, s76, 7868416\n"
"s_mov_b32 s90, 0xffff\n"
"s_mov_b32 s91, 0xffff\n"
"v_mov_b32 v164, 0xdddd\n"
"v_and_b32 v4, 27, v4\n"
"v_cmp_eq_u32 s[30:31], 16, v4\n"
"v_mov_b32 v5, 0x3f803f80\n"
"s_nop 0\n"
"v_cndmask_b32 v4, 0, v5, s[30:31]\n"
"v_mov_b32 v5, v4\n"
"v_pk_mov_b32 v[148:149], v[4:5], v[4:5]\n"
"v_pk_mov_b32 v[150:151], v[4:5], v[4:5]\n"
"v_pk_mov_b32 v[152:153], v[4:5], v[4:5]\n"
"v_pk_mov_b32 v[154:155], v[4:5], v[4:5]\n"
"v_pk_mov_b32 v[156:157], v[4:5], v[4:5]\n"
"v_pk_mov_b32 v[158:159], v[4:5], v[4:5]\n"
"v_pk_mov_b32 v[160:161], v[4:5], v[4:5]\n"
"v_pk_mov_b32 v[162:163], v[4:5], v[4:5]\n"
"v_mov_b32 v4, 0\n"
"v_mov_b32 v5, 0\n"
"v_pk_mov_b32 v[100:101], v[4:5], v[4:5]\n"
"v_pk_mov_b32 v[102:103], v[4:5], v[4:5]\n"
"v_pk_mov_b32 v[104:105], v[4:5], v[4:5]\n"
"v_pk_mov_b32 v[106:107], v[4:5], v[4:5]\n"
"v_pk_mov_b32 v[108:109], v[4:5], v[4:5]\n"
"v_pk_mov_b32 v[110:111], v[4:5], v[4:5]\n"
"v_pk_mov_b32 v[112:113], v[4:5], v[4:5]\n"
"v_pk_mov_b32 v[114:115], v[4:5], v[4:5]\n"
"v_pk_mov_b32 v[116:117], v[4:5], v[4:5]\n"
"v_pk_mov_b32 v[118:119], v[4:5], v[4:5]\n"
"v_pk_mov_b32 v[120:121], v[4:5], v[4:5]\n"
"v_pk_mov_b32 v[122:123], v[4:5], v[4:5]\n"
"v_pk_mov_b32 v[124:125], v[4:5], v[4:5]\n"
"v_pk_mov_b32 v[126:127], v[4:5], v[4:5]\n"
"v_pk_mov_b32 v[128:129], v[4:5], v[4:5]\n"
"v_pk_mov_b32 v[130:131], v[4:5], v[4:5]\n"
"Lcls_27_%=:\n"
"s_waitcnt vmcnt(0)\n"
"s_barrier\n"
"s_cmp_lg_u32 s68, 0\n"
"s_cbranch_scc1 Lnokvflag_28_%=\n"
"s_mov_b64 exec, 1\n"
"buffer_store_dwordx2 v[52:53], v54, s[40:43], s98 offen sc1\n"
"s_mov_b64 exec, -1\n"
"Lnokvflag_28_%=:\n"
"s_branch Lkvend_22_%=\n"
"Ljn_23_%=:\n"
"s_cmp_ge_u32 s68, 4\n"
"s_cbranch_scc1 Lcls_29_%=\n"
"v_and_b32 v4, 31, v1\n"
"v_lshrrev_b32 v5, 5, v1\n"
"v_add_u32 v236, 65536, v2\n"
"v_add_u32 v237, 98304, v2\n"
"v_lshlrev_b32 v5, 8, v5\n"
"v_lshl_add_u32 v5, v4, 4, v5\n"
"v_add_u32 v238, 66560, v5\n"
"v_add_u32 v239, 99328, v5\n"
"s_lshr_b32 s75, s71, 1\n"
"s_lshl_b32 s75, s75, 14\n"
"s_and_b32 s76, s71, 1\n"
"s_lshl_b32 s76, s76, 10\n"
"s_add_u32 s75, s75, s76\n"
"s_add_u32 s75, s75, 110592\n"
"v_add_u32 v240, s75, v2\n"
"s_lshl_b32 s75, s71, 13\n"
"s_add_u32 s75, s75, 131072\n"
"v_add_u32 v242, s75, v2\n"
"v_lshl_add_u32 v5, v1, 2, s75\n"
"v_add_u32 v241, 7680, v5\n"
"v_add_u32 v229, 4096, v5\n"
"v_bfe_u32 v5, v1, 4, 1\n"
"v_lshlrev_b32 v225, 13, v5\n"
"v_lshrrev_b32 v5, 5, v1\n"
"v_lshl_add_u32 v225, v5, 8, v225\n"
"v_and_b32 v5, 15, v1\n"
"v_lshl_add_u32 v225, v5, 4, v225\n"
"v_add_u32 v225, 138240, v225\n"
"v_lshrrev_b32 v5, 3, v1\n"
"s_lshl_b32 s75, s70, 3\n"
"v_add_lshl_u32 v246, v5, s75, 7\n"
"v_and_b32 v5, 7, v1\n"
"v_lshl_add_u32 v246, v5, 3, v246\n"
"v_mov_b32 v204, s72\n"
"v_mov_b32 v205, s73\n"
"v_mov_b32 v206, 0\n"
"s_lshl_b32 s76, s70, 3\n"
"s_add_u32 s76, s76, s69\n"
"s_lshl_b32 s76, s76, 4\n"
"s_add_u32 s76, s76, s68\n"
"s_lshl_b32 s76, s76, 3\n"
"s_add_u32 s85, s76, 7868416\n"
"s_mov_b32 s90, 0xffff\n"
"s_mov_b32 s91, 0xffff\n"
"v_mov_b32 v164, 0xdddd\n"
"v_and_b32 v4, 27, v4\n"
"v_cmp_eq_u32 s[30:31], 16, v4\n"
"v_mov_b32 v5, 0x3f803f80\n"
"s_nop 0\n"
"v_cndmask_b32 v4, 0, v5, s[30:31]\n"
"v_mov_b32 v5, v4\n"
"v_pk_mov_b32 v[148:149], v[4:5], v[4:5]\n"
"v_pk_mov_b32 v[150:151], v[4:5], v[4:5]\n"
"v_pk_mov_b32 v[152:153], v[4:5], v[4:5]\n"
"v_pk_mov_b32 v[154:155], v[4:5], v[4:5]\n"
"v_pk_mov_b32 v[156:157], v[4:5], v[4:5]\n"
"v_pk_mov_b32 v[158:159], v[4:5], v[4:5]\n"
"v_pk_mov_b32 v[160:161], v[4:5], v[4:5]\n"
"v_pk_mov_b32 v[162:163], v[4:5], v[4:5]\n"
"v_mov_b32 v4, 0\n"
"v_mov_b32 v5, 0\n"
"v_pk_mov_b32 v[100:101], v[4:5], v[4:5]\n"
"v_pk_mov_b32 v[102:103], v[4:5], v[4:5]\n"
"v_pk_mov_b32 v[104:105], v[4:5], v[4:5]\n"
"v_pk_mov_b32 v[106:107], v[4:5], v[4:5]\n"
"v_pk_mov_b32 v[108:109], v[4:5], v[4:5]\n"
"v_pk_mov_b32 v[110:111], v[4:5], v[4:5]\n"
"v_pk_mov_b32 v[112:113], v[4:5], v[4:5]\n"
"v_pk_mov_b32 v[114:115], v[4:5], v[4:5]\n"
"v_pk_mov_b32 v[116:117], v[4:5], v[4:5]\n"
"v_pk_mov_b32 v[118:119], v[4:5], v[4:5]\n"
"v_pk_mov_b32 v[120:121], v[4:5], v[4:5]\n"
"v_pk_mov_b32 v[122:123], v[4:5], v[4:5]\n"
"v_pk_mov_b32 v[124:125], v[4:5], v[4:5]\n"
"v_pk_mov_b32 v[126:127], v[4:5], v[4:5]\n"
"v_pk_mov_b32 v[128:129], v[4:5], v[4:5]\n"
"v_pk_mov_b32 v[130:131], v[4:5], v[4:5]\n"
"Lcls_29_%=:\n"
"Lkvend_22_%=:\n"
"s_cmp_ge_u32 s68, 4\n"
"s_cbranch_scc1 Lbstart_30_%=\n"
"s_setprio 2\n"
"s_mov_b32 s81, 0\n"
"s_cbranch_vccz Lanotile_33_%=\n"
"s_add_u32 m0, s80, 8192\n"
"s_nop 0\n"
"buffer_load_dwordx4 v226, s[36:39], s79 offen lds\n"
"s_add_u32 s79, s79, s78\n"
"s_add_u32 m0, s80, 9216\n"
"s_nop 0\n"
"buffer_load_dwordx4 v226, s[36:39], s79 offen lds\n"
"s_add_u32 s79, s79, s78\n"
"s_add_u32 m0, s80, 10240\n"
"s_nop 0\n"
"buffer_load_dwordx4 v226, s[36:39], s79 offen lds\n"
"s_add_u32 s79, s79, s78\n"
"s_add_u32 m0, s80, 11264\n"
"s_nop 0\n"
"buffer_load_dwordx4 v226, s[36:39], s79 offen lds\n"
"s_add_u32 s79, s79, s78\n"
"s_add_u32 m0, s80, 12288\n"
"s_nop 0\n"
"buffer_load_dwordx4 v226, s[36:39], s79 offen lds\n"
"s_add_u32 s79, s79, s78\n"
"s_add_u32 m0, s80, 13312\n"
"s_nop 0\n"
"buffer_load_dwordx4 v226, s[36:39], s79 offen lds\n"
"s_add_u32 s79, s79, s78\n"
"s_add_u32 m0, s80, 14336\n"
"s_nop 0\n"
"buffer_load_dwordx4 v226, s[36:39], s79 offen lds\n"
"s_add_u32 s79, s79, s78\n"
"s_add_u32 m0, s80, 15360\n"
"s_nop 0\n"
"buffer_load_dwordx4 v226, s[36:39], s79 offen lds\n"
"s_add_u32 s79, s79, s78\n"
"s_cmp_eq_u32 s60, 2\n"
"s_cselect_b32 s79, s61, s79\n"
"s_waitcnt vmcnt(16)\n"
"s_waitcnt lgkmcnt(0)\n"
"v_cmp_gt_u32 s[30:31], 32, v1\n"
"v_mov_b32 v56, v227\n"
"v_mov_b32 v57, 0\n"
"v_pk_mov_b32 v[58:59], v[56:57], v[56:57] op_sel:[1,1]\n"
"v_lshrrev_b32 v6, 16, v14\n"
"v_cndmask_b32 v76, 0, v6, s[30:31]\n"
"v_mov_b32 v77, 0\n"
"v_pk_mov_b32 v[78:79], v[58:59], v[58:59]\n"
"v_lshrrev_b32 v6, 16, v15\n"
"v_cndmask_b32 v80, 0, v6, s[30:31]\n"
"v_mov_b32 v81, 0\n"
"v_pk_mov_b32 v[82:83], v[58:59], v[58:59]\n"
"v_mfma_f32_32x32x16_bf16 v[16:31], v[56:59], v[76:79], 0\n"
"s_nop 0\n"
"v_mfma_f32_32x32x16_bf16 v[32:47], v[56:59], v[80:83], 0\n"
"s_waitcnt vmcnt(15)\n"
"ds_read_b64_tr_b16 v[48:49], v228 offset:0\n"
"ds_read_b64_tr_b16 v[50:51], v228 offset:256\n"
"ds_read_b128 v[60:63], v232 offset:0\n"
"ds_read_b128 v[64:67], v232 offset:32768\n"
"s_waitcnt vmcnt(14)\n"
"ds_read_b64_tr_b16 v[52:53], v228 offset:1024\n"
"ds_read_b64_tr_b16 v[54:55], v228 offset:1280\n"
"ds_read_b128 v[68:71], v233 offset:0\n"
"ds_read_b128 v[72:75], v233 offset:32768\n"
"s_waitcnt vmcnt(13) lgkmcnt(4)\n"
"s_add_u32 m0, s80, 0\n"
"v_mfma_f32_32x32x16_bf16 v[16:31], v[48:51], v[60:63], v[16:31]\n"
"ds_read_b64_tr_b16 v[56:57], v228 offset:2048\n"
"ds_read_b64_tr_b16 v[58:59], v228 offset:2304\n"
"ds_read_b128 v[76:79], v234 offset:0\n"
"ds_read_b128 v[80:83], v234 offset:32768\n"
"buffer_load_dwordx4 v226, s[36:39], s79 offen lds\n"
"s_add_u32 s79, s79, s78\n"
"v_mfma_f32_32x32x16_bf16 v[32:47], v[48:51], v[64:67], v[32:47]\n"
"s_waitcnt vmcnt(13) lgkmcnt(4)\n"
"s_add_u32 m0, s80, 1024\n"
"v_mfma_f32_32x32x16_bf16 v[16:31], v[52:55], v[68:71], v[16:31]\n"
"ds_read_b64_tr_b16 v[48:49], v228 offset:3072\n"
"ds_read_b64_tr_b16 v[50:51], v228 offset:3328\n"
"ds_read_b128 v[60:63], v235 offset:0\n"
"ds_read_b128 v[64:67], v235 offset:32768\n"
"buffer_load_dwordx4 v226, s[36:39], s79 offen lds\n"
"s_add_u32 s79, s79, s78\n"
"v_mfma_f32_32x32x16_bf16 v[32:47], v[52:55], v[72:75], v[32:47]\n"
"s_waitcnt vmcnt(13) lgkmcnt(4)\n"
"s_add_u32 m0, s80, 2048\n"
"v_mfma_f32_32x32x16_bf16 v[16:31], v[56:59], v[76:79], v[16:31]\n"
"ds_read_b64_tr_b16 v[52:53], v228 offset:4096\n"
"ds_read_b64_tr_b16 v[54:55], v228 offset:4352\n"
"ds_read_b128 v[68:71], v232 offset:4096\n"
"ds_read_b128 v[72:75], v232 offset:36864\n"
"buffer_load_dwordx4 v226, s[36:39], s79 offen lds\n"
"s_add_u32 s79, s79, s78\n"
"v_mfma_f32_32x32x16_bf16 v[32:47], v[56:59], v[80:83], v[32:47]\n"
"s_waitcnt vmcnt(13) lgkmcnt(4)\n"
"s_add_u32 m0, s80, 3072\n"
"v_mfma_f32_32x32x16_bf16 v[16:31], v[48:51], v[60:63], v[16:31]\n"
"ds_read_b64_tr_b16 v[56:57], v228 offset:5120\n"
"ds_read_b64_tr_b16 v[58:59], v228 offset:5376\n"
"ds_read_b128 v[76:79], v233 offset:4096\n"
"ds_read_b128 v[80:83], v233 offset:36864\n"
"buffer_load_dwordx4 v226, s[36:39], s79 offen lds\n"
"s_add_u32 s79, s79, s78\n"
"v_mfma_f32_32x32x16_bf16 v[32:47], v[48:51], v[64:67], v[32:47]\n"
"s_waitcnt vmcnt(13) lgkmcnt(4)\n"
"s_add_u32 m0, s80, 4096\n"
"v_mfma_f32_32x32x16_bf16 v[16:31], v[52:55], v[68:71], v[16:31]\n"
"ds_read_b64_tr_b16 v[48:49], v228 offset:6144\n"
"ds_read_b64_tr_b16 v[50:51], v228 offset:6400\n"
"ds_read_b128 v[60:63], v234 offset:4096\n"
"ds_read_b128 v[64:67], v234 offset:36864\n"
"buffer_load_dwordx4 v226, s[36:39], s79 offen lds\n"
"s_add_u32 s79, s79, s78\n"
"v_mfma_f32_32x32x16_bf16 v[32:47], v[52:55], v[72:75], v[32:47]\n"
"s_waitcnt vmcnt(13) lgkmcnt(4)\n"
"s_add_u32 m0, s80, 5120\n"
"v_mfma_f32_32x32x16_bf16 v[16:31], v[56:59], v[76:79], v[16:31]\n"
"ds_read_b64_tr_b16 v[52:53], v228 offset:7168\n"
"ds_read_b64_tr_b16 v[54:55], v228 offset:7424\n"
"ds_read_b128 v[68:71], v235 offset:4096\n"
"ds_read_b128 v[72:75], v235 offset:36864\n"
"buffer_load_dwordx4 v226, s[36:39], s79 offen lds\n"
"s_add_u32 s79, s79, s78\n"
"v_mfma_f32_32x32x16_bf16 v[32:47], v[56:59], v[80:83], v[32:47]\n"
"s_waitcnt vmcnt(13) lgkmcnt(4)\n"
"s_add_u32 m0, s80, 6144\n"
"v_mfma_f32_32x32x16_bf16 v[16:31], v[48:51], v[60:63], v[16:31]\n"
"ds_read_b64_tr_b16 v[56:57], v228 offset:8192\n"
"ds_read_b64_tr_b16 v[58:59], v228 offset:8448\n"
"s_cmp_eq_u32 s60, 3\n"
"s_mov_b32 s75, 8192\n"
"s_cselect_b32 s75, -24576, s75\n"
"v_add_u32 v232, s75, v232\n"
"v_add_u32 v233, s75, v233\n"
"v_add_u32 v234, s75, v234\n"
"v_add_u32 v235, s75, v235\n"
"ds_read_b128 v[76:79], v232 offset:0\n"
"ds_read_b128 v[80:83], v232 offset:32768\n"
"buffer_load_dwordx4 v226, s[36:39], s79 offen lds\n"
"s_add_u32 s79, s79, s78\n"
"v_mfma_f32_32x32x16_bf16 v[32:47], v[48:51], v[64:67], v[32:47]\n"
"s_waitcnt vmcnt(13) lgkmcnt(4)\n"
"s_add_u32 m0, s80, 7168\n"
"v_mfma_f32_32x32x16_bf16 v[16:31], v[52:55], v[68:71], v[16:31]\n"
"ds_read_b64_tr_b16 v[48:49], v228 offset:9216\n"
"ds_read_b64_tr_b16 v[50:51], v228 offset:9472\n"
"ds_read_b128 v[60:63], v233 offset:0\n"
"ds_read_b128 v[64:67], v233 offset:32768\n"
"buffer_load_dwordx4 v226, s[36:39], s79 offen lds\n"
"s_add_u32 s79, s79, s78\n"
"s_cmp_eq_u32 s60, 1\n"
"s_cselect_b32 s79, s61, s79\n"
"v_mfma_f32_32x32x16_bf16 v[32:47], v[52:55], v[72:75], v[32:47]\n"
"s_waitcnt vmcnt(13) lgkmcnt(4)\n"
"s_add_u32 m0, s80, 8192\n"
"v_mfma_f32_32x32x16_bf16 v[16:31], v[56:59], v[76:79], v[16:31]\n"
"ds_read_b64_tr_b16 v[52:53], v228 offset:10240\n"
"ds_read_b64_tr_b16 v[54:55], v228 offset:10496\n"
"ds_read_b128 v[68:71], v234 offset:0\n"
"ds_read_b128 v[72:75], v234 offset:32768\n"
"buffer_load_dwordx4 v226, s[36:39], s79 offen lds\n"
"s_add_u32 s79, s79, s78\n"
"v_mfma_f32_32x32x16_bf16 v[32:47], v[56:59], v[80:83], v[32:47]\n"
"s_waitcnt vmcnt(13) lgkmcnt(4)\n"
"s_add_u32 m0, s80, 9216\n"
"v_mfma_f32_32x32x16_bf16 v[16:31], v[48:51], v[60:63], v[16:31]\n"
"ds_read_b64_tr_b16 v[56:57], v228 offset:11264\n"
"ds_read_b64_tr_b16 v[58:59], v228 offset:11520\n"
"ds_read_b128 v[76:79], v235 offset:0\n"
"ds_read_b128 v[80:83], v235 offset:32768\n"
"buffer_load_dwordx4 v226, s[36:39], s79 offen lds\n"
"s_add_u32 s79, s79, s78\n"
"v_mfma_f32_32x32x16_bf16 v[32:47], v[48:51], v[64:67], v[32:47]\n"
"s_waitcnt vmcnt(13) lgkmcnt(4)\n"
"s_add_u32 m0, s80, 10240\n"
"v_mfma_f32_32x32x16_bf16 v[16:31], v[52:55], v[68:71], v[16:31]\n"
"ds_read_b64_tr_b16 v[48:49], v228 offset:12288\n"
"ds_read_b64_tr_b16 v[50:51], v228 offset:12544\n"
"ds_read_b128 v[60:63], v232 offset:4096\n"
"ds_read_b128 v[64:67], v232 offset:36864\n"
"buffer_load_dwordx4 v226, s[36:39], s79 offen lds\n"
"s_add_u32 s79, s79, s78\n"
"v_mfma_f32_32x32x16_bf16 v[32:47], v[52:55], v[72:75], v[32:47]\n"
"s_waitcnt vmcnt(13) lgkmcnt(4)\n"
"s_add_u32 m0, s80, 11264\n"
"v_mfma_f32_32x32x16_bf16 v[16:31], v[56:59], v[76:79], v[16:31]\n"
"ds_read_b64_tr_b16 v[52:53], v228 offset:13312\n"
"ds_read_b64_tr_b16 v[54:55], v228 offset:13568\n"
"ds_read_b128 v[68:71], v233 offset:4096\n"
"ds_read_b128 v[72:75], v233 offset:36864\n"
"buffer_load_dwordx4 v226, s[36:39], s79 offen lds\n"
"s_add_u32 s79, s79, s78\n"
"v_mfma_f32_32x32x16_bf16 v[32:47], v[56:59], v[80:83], v[32:47]\n"
"s_waitcnt vmcnt(13) lgkmcnt(4)\n"
"s_add_u32 m0, s80, 12288\n"
"v_mfma_f32_32x32x16_bf16 v[16:31], v[48:51], v[60:63], v[16:31]\n"
"ds_read_b64_tr_b16 v[56:57], v228 offset:14336\n"
"ds_read_b64_tr_b16 v[58:59], v228 offset:14592\n"
"ds_read_b128 v[76:79], v234 offset:4096\n"
"ds_read_b128 v[80:83], v234 offset:36864\n"
"buffer_load_dwordx4 v226, s[36:39], s79 offen lds\n"
"s_add_u32 s79, s79, s78\n"
"v_mfma_f32_32x32x16_bf16 v[32:47], v[48:51], v[64:67], v[32:47]\n"
"s_waitcnt vmcnt(13) lgkmcnt(4)\n"
"s_add_u32 m0, s80, 13312\n"
"v_mfma_f32_32x32x16_bf16 v[16:31], v[52:55], v[68:71], v[16:31]\n"
"ds_read_b64_tr_b16 v[48:49], v228 offset:15360\n"
"ds_read_b64_tr_b16 v[50:51], v228 offset:15616\n"
"ds_read_b128 v[60:63], v235 offset:4096\n"
"ds_read_b128 v[64:67], v235 offset:36864\n"
"buffer_load_dwordx4 v226, s[36:39], s79 offen lds\n"
"s_add_u32 s79, s79, s78\n"
"v_mfma_f32_32x32x16_bf16 v[32:47], v[52:55], v[72:75], v[32:47]\n"
"s_waitcnt vmcnt(13) lgkmcnt(4)\n"
"s_add_u32 m0, s80, 14336\n"
"v_mfma_f32_32x32x16_bf16 v[16:31], v[56:59], v[76:79], v[16:31]\n"
"ds_read_b64_tr_b16 v[52:53], v228 offset:0\n"
"ds_read_b64_tr_b16 v[54:55], v228 offset:256\n"
"s_cmp_eq_u32 s60, 2\n"
"s_mov_b32 s75, 8192\n"
"s_cselect_b32 s75, -24576, s75\n"
"v_add_u32 v232, s75, v232\n"
"v_add_u32 v233, s75, v233\n"
"v_add_u32 v234, s75, v234\n"
"v_add_u32 v235, s75, v235\n"
"ds_read_b128 v[68:71], v232 offset:0\n"
"ds_read_b128 v[72:75], v232 offset:32768\n"
"buffer_load_dwordx4 v226, s[36:39], s79 offen lds\n"
"s_add_u32 s79, s79, s78\n"
"v_mfma_f32_32x32x16_bf16 v[32:47], v[56:59], v[80:83], v[32:47]\n"
"s_waitcnt vmcnt(13) lgkmcnt(4)\n"
"s_add_u32 m0, s80, 15360\n"
"v_mfma_f32_32x32x16_bf16 v[16:31], v[48:51], v[60:63], v[16:31]\n"
"ds_read_b64_tr_b16 v[56:57], v228 offset:1024\n"
"ds_read_b64_tr_b16 v[58:59], v228 offset:1280\n"
"ds_read_b128 v[76:79], v233 offset:0\n"
"ds_read_b128 v[80:83], v233 offset:32768\n"
"buffer_load_dwordx4 v226, s[36:39], s79 offen lds\n"
"s_add_u32 s79, s79, s78\n"
"v_mfma_f32_32x32x16_bf16 v[32:47], v[48:51], v[64:67], v[32:47]\n"
"s_waitcnt vmcnt(13) lgkmcnt(4)\n"
"v_mfma_f32_32x32x16_bf16 v[16:31], v[52:55], v[68:71], v[16:31]\n"
"ds_read_b64_tr_b16 v[48:49], v228 offset:2048\n"
"ds_read_b64_tr_b16 v[50:51], v228 offset:2304\n"
"ds_read_b128 v[60:63], v234 offset:0\n"
"ds_read_b128 v[64:67], v234 offset:32768\n"
"v_mfma_f32_32x32x16_bf16 v[32:47], v[52:55], v[72:75], v[32:47]\n"
"s_waitcnt vmcnt(12) lgkmcnt(4)\n"
"v_mfma_f32_32x32x16_bf16 v[16:31], v[56:59], v[76:79], v[16:31]\n"
"ds_read_b64_tr_b16 v[52:53], v228 offset:3072\n"
"ds_read_b64_tr_b16 v[54:55], v228 offset:3328\n"
"ds_read_b128 v[68:71], v235 offset:0\n"
"ds_read_b128 v[72:75], v235 offset:32768\n"
"v_mfma_f32_32x32x16_bf16 v[32:47], v[56:59], v[80:83], v[32:47]\n"
"s_mov_b64 exec, 15\n"
"buffer_load_dwordx2 v[166:167], v231, s[40:43], s82 offen sc1\n"
"s_mov_b64 exec, -1\n"
"s_waitcnt vmcnt(12) lgkmcnt(4)\n"
"v_mfma_f32_32x32x16_bf16 v[16:31], v[48:51], v[60:63], v[16:31]\n"
"ds_read_b64_tr_b16 v[56:57], v228 offset:4096\n"
"ds_read_b64_tr_b16 v[58:59], v228 offset:4352\n"
"ds_read_b128 v[76:79], v232 offset:4096\n"
"ds_read_b128 v[80:83], v232 offset:36864\n"
"v_mfma_f32_32x32x16_bf16 v[32:47], v[48:51], v[64:67], v[32:47]\n"
"s_waitcnt vmcnt(11) lgkmcnt(4)\n"
"v_mfma_f32_32x32x16_bf16 v[16:31], v[52:55], v[68:71], v[16:31]\n"
"ds_read_b64_tr_b16 v[48:49], v228 offset:5120\n"
"ds_read_b64_tr_b16 v[50:51], v228 offset:5376\n"
"ds_read_b128 v[60:63], v233 offset:4096\n"
"ds_read_b128 v[64:67], v233 offset:36864\n"
"v_mfma_f32_32x32x16_bf16 v[32:47], v[52:55], v[72:75], v[32:47]\n"
"s_waitcnt vmcnt(10) lgkmcnt(4)\n"
"v_mfma_f32_32x32x16_bf16 v[16:31], v[56:59], v[76:79], v[16:31]\n"
"ds_read_b64_tr_b16 v[52:53], v228 offset:6144\n"
"ds_read_b64_tr_b16 v[54:55], v228 offset:6400\n"
"ds_read_b128 v[68:71], v234 offset:4096\n"
"ds_read_b128 v[72:75], v234 offset:36864\n"
"v_mfma_f32_32x32x16_bf16 v[32:47], v[56:59], v[80:83], v[32:47]\n"
"s_waitcnt vmcnt(9) lgkmcnt(4)\n"
"v_mfma_f32_32x32x16_bf16 v[16:31], v[48:51], v[60:63], v[16:31]\n"
"ds_read_b64_tr_b16 v[56:57], v228 offset:7168\n"
"ds_read_b64_tr_b16 v[58:59], v228 offset:7424\n"
"ds_read_b128 v[76:79], v235 offset:4096\n"
"ds_read_b128 v[80:83], v235 offset:36864\n"
"v_mfma_f32_32x32x16_bf16 v[32:47], v[48:51], v[64:67], v[32:47]\n"
"s_waitcnt vmcnt(8) lgkmcnt(4)\n"
"v_mfma_f32_32x32x16_bf16 v[16:31], v[52:55], v[68:71], v[16:31]\n"
"ds_read_b64_tr_b16 v[48:49], v228 offset:8192\n"
"ds_read_b64_tr_b16 v[50:51], v228 offset:8448\n"
"s_cmp_eq_u32 s60, 1\n"
"s_mov_b32 s75, 8192\n"
"s_cselect_b32 s75, -24576, s75\n"
"v_add_u32 v232, s75, v232\n"
"v_add_u32 v233, s75, v233\n"
"v_add_u32 v234, s75, v234\n"
"v_add_u32 v235, s75, v235\n"
"ds_read_b128 v[60:63], v232 offset:0\n"
"ds_read_b128 v[64:67], v232 offset:32768\n"
"v_mfma_f32_32x32x16_bf16 v[32:47], v[52:55], v[72:75], v[32:47]\n"
"s_waitcnt vmcnt(7) lgkmcnt(4)\n"
"v_mfma_f32_32x32x16_bf16 v[16:31], v[56:59], v[76:79], v[16:31]\n"
"ds_read_b64_tr_b16 v[52:53], v228 offset:9216\n"
"ds_read_b64_tr_b16 v[54:55], v228 offset:9472\n"
"ds_read_b128 v[68:71], v233 offset:0\n"
"ds_read_b128 v[72:75], v233 offset:32768\n"
"v_mfma_f32_32x32x16_bf16 v[32:47], v[56:59], v[80:83], v[32:47]\n"
"s_waitcnt vmcnt(6) lgkmcnt(4)\n"
"v_mfma_f32_32x32x16_bf16 v[16:31], v[48:51], v[60:63], v[16:31]\n"
"ds_read_b64_tr_b16 v[56:57], v228 offset:10240\n"
"ds_read_b64_tr_b16 v[58:59], v228 offset:10496\n"
"ds_read_b128 v[76:79], v234 offset:0\n"
"ds_read_b128 v[80:83], v234 offset:32768\n"
"v_mfma_f32_32x32x16_bf16 v[32:47], v[48:51], v[64:67], v[32:47]\n"
"s_waitcnt vmcnt(5) lgkmcnt(4)\n"
"v_mfma_f32_32x32x16_bf16 v[16:31], v[52:55], v[68:71], v[16:31]\n"
"ds_read_b64_tr_b16 v[48:49], v228 offset:11264\n"
"ds_read_b64_tr_b16 v[50:51], v228 offset:11520\n"
"ds_read_b128 v[60:63], v235 offset:0\n"
"ds_read_b128 v[64:67], v235 offset:32768\n"
"v_mfma_f32_32x32x16_bf16 v[32:47], v[52:55], v[72:75], v[32:47]\n"
"s_waitcnt vmcnt(0)\n"
"s_mov_b64 exec, 15\n"
"v_cmp_ne_u64 vcc, s[72:73], v[166:167]\n"
"s_mov_b64 exec, -1\n"
"s_cbranch_vccnz Lkvskip_58_%=\n"
"s_lshl_b32 s75, s71, 13\n"
"s_add_u32 m0, s75, 138240\n"
"s_lshr_b32 s75, s75, 4\n"
"s_add_u32 s75, s75, 7901184\n"
"s_mov_b32 exec_hi, 0\n"
"buffer_load_dwordx4 v2, s[40:43], s75 offen offset:0 sc1 lds\n"
"s_mov_b32 exec_hi, -1\n"
"s_add_u32 m0, s80, 0\n"
"s_add_u32 s75, s83, 0\n"
"buffer_load_dwordx4 v2, s[40:43], s75 offen offset:0 sc1 lds\n"
"buffer_load_dwordx4 v2, s[40:43], s75 offen offset:1024 sc1 lds\n"
"s_add_u32 m0, s80, 0\n"
"s_add_u32 s75, s83, 0\n"
"buffer_load_dwordx4 v2, s[40:43], s75 offen offset:2048 sc1 lds\n"
"buffer_load_dwordx4 v2, s[40:43], s75 offen offset:3072 sc1 lds\n"
"s_mov_b32 s81, 1\n"
"s_branch Lkvdone_59_%=\n"
"Lkvskip_58_%=:\n"
"s_mov_b64 exec, 15\n"
"buffer_load_dwordx2 v[166:167], v231, s[40:43], s82 offen sc1\n"
"s_mov_b64 exec, -1\n"
"Lkvdone_59_%=:\n"
"s_waitcnt lgkmcnt(4)\n"
"v_mfma_f32_32x32x16_bf16 v[16:31], v[56:59], v[76:79], v[16:31]\n"
"ds_read_b64_tr_b16 v[52:53], v228 offset:12288\n"
"ds_read_b64_tr_b16 v[54:55], v228 offset:12544\n"
"ds_read_b128 v[68:71], v232 offset:4096\n"
"ds_read_b128 v[72:75], v232 offset:36864\n"
"v_mfma_f32_32x32x16_bf16 v[32:47], v[56:59], v[80:83], v[32:47]\n"
"s_waitcnt lgkmcnt(4)\n"
"v_mfma_f32_32x32x16_bf16 v[16:31], v[48:51], v[60:63], v[16:31]\n"
"ds_read_b64_tr_b16 v[56:57], v228 offset:13312\n"
"ds_read_b64_tr_b16 v[58:59], v228 offset:13568\n"
"ds_read_b128 v[76:79], v233 offset:4096\n"
"ds_read_b128 v[80:83], v233 offset:36864\n"
"v_mfma_f32_32x32x16_bf16 v[32:47], v[48:51], v[64:67], v[32:47]\n"
"s_waitcnt lgkmcnt(4)\n"
"v_mfma_f32_32x32x16_bf16 v[16:31], v[52:55], v[68:71], v[16:31]\n"
"ds_read_b64_tr_b16 v[48:49], v228 offset:14336\n"
"ds_read_b64_tr_b16 v[50:51], v228 offset:14592\n"
"ds_read_b128 v[60:63], v234 offset:4096\n"
"ds_read_b128 v[64:67], v234 offset:36864\n"
"v_mfma_f32_32x32x16_bf16 v[32:47], v[52:55], v[72:75], v[32:47]\n"
"s_waitcnt lgkmcnt(4)\n"
"v_mfma_f32_32x32x16_bf16 v[16:31], v[56:59], v[76:79], v[16:31]\n"
"ds_read_b64_tr_b16 v[52:53], v228 offset:15360\n"
"ds_read_b64_tr_b16 v[54:55], v228 offset:15616\n"
"ds_read_b128 v[68:71], v235 offset:4096\n"
"ds_read_b128 v[72:75], v235 offset:36864\n"
"v_mfma_f32_32x32x16_bf16 v[32:47], v[56:59], v[80:83], v[32:47]\n"
"s_waitcnt lgkmcnt(4)\n"
"v_mfma_f32_32x32x16_bf16 v[16:31], v[48:51], v[60:63], v[16:31]\n"
"v_mfma_f32_32x32x16_bf16 v[32:47], v[48:51], v[64:67], v[32:47]\n"
"s_waitcnt lgkmcnt(0)\n"
"v_mfma_f32_32x32x16_bf16 v[16:31], v[52:55], v[68:71], v[16:31]\n"
"v_mfma_f32_32x32x16_bf16 v[32:47], v[52:55], v[72:75], v[32:47]\n"
"s_cmp_ge_u32 s71, 2\n"
"s_cbranch_scc1 Lqepi_60_%=\n"
"s_add_u32 s75, s84, 88064\n"
"s_nop 7\n"
"v_mul_f32 v16, v16, |v16|\n"
"v_mul_f32 v17, v17, |v17|\n"
"v_mul_f32 v18, v18, |v18|\n"
"v_mul_f32 v19, v19, |v19|\n"
"v_mul_f32 v20, v20, |v20|\n"
"v_mul_f32 v21, v21, |v21|\n"
"v_mul_f32 v22, v22, |v22|\n"
"v_mul_f32 v23, v23, |v23|\n"
"v_cvt_pk_bf16_f32 v84, v16, v17\n"
"v_cvt_pk_bf16_f32 v85, v18, v19\n"
"v_cvt_pk_bf16_f32 v86, v20, v21\n"
"v_cvt_pk_bf16_f32 v87, v22, v23\n"
"v_pk_max_i16 v84, v84, 0\n"
"v_pk_max_i16 v85, v85, 0\n"
"v_pk_max_i16 v86, v86, 0\n"
"v_pk_max_i16 v87, v87, 0\n"
"buffer_store_dwordx4 v[84:87], v2, s[40:43], s84 offen offset:0 sc1\n"
"v_mul_f32 v24, v24, |v24|\n"
"v_mul_f32 v25, v25, |v25|\n"
"v_mul_f32 v26, v26, |v26|\n"
"v_mul_f32 v27, v27, |v27|\n"
"v_mul_f32 v28, v28, |v28|\n"
"v_mul_f32 v29, v29, |v29|\n"
"v_mul_f32 v30, v30, |v30|\n"
"v_mul_f32 v31, v31, |v31|\n"
"v_cvt_pk_bf16_f32 v88, v24, v25\n"
"v_cvt_pk_bf16_f32 v89, v26, v27\n"
"v_cvt_pk_bf16_f32 v90, v28, v29\n"
"v_cvt_pk_bf16_f32 v91, v30, v31\n"
"v_pk_max_i16 v88, v88, 0\n"
"v_pk_max_i16 v89, v89, 0\n"
"v_pk_max_i16 v90, v90, 0\n"
"v_pk_max_i16 v91, v91, 0\n"
"buffer_store_dwordx4 v[88:91], v2, s[40:43], s84 offen offset:1024 sc1\n"
"v_mul_f32 v32, v32, |v32|\n"
"v_mul_f32 v33, v33, |v33|\n"
"v_mul_f32 v34, v34, |v34|\n"
"v_mul_f32 v35, v35, |v35|\n"
"v_mul_f32 v36, v36, |v36|\n"
"v_mul_f32 v37, v37, |v37|\n"
"v_mul_f32 v38, v38, |v38|\n"
"v_mul_f32 v39, v39, |v39|\n"
"v_cvt_pk_bf16_f32 v92, v32, v33\n"
"v_cvt_pk_bf16_f32 v93, v34, v35\n"
"v_cvt_pk_bf16_f32 v94, v36, v37\n"
"v_cvt_pk_bf16_f32 v95, v38, v39\n"
"v_pk_max_i16 v92, v92, 0\n"
"v_pk_max_i16 v93, v93, 0\n"
"v_pk_max_i16 v94, v94, 0\n"
"v_pk_max_i16 v95, v95, 0\n"
"buffer_store_dwordx4 v[92:95], v2, s[40:43], s75 offen offset:0 sc1\n"
"v_mul_f32 v40, v40, |v40|\n"
"v_mul_f32 v41, v41, |v41|\n"
"v_mul_f32 v42, v42, |v42|\n"
"v_mul_f32 v43, v43, |v43|\n"
"v_mul_f32 v44, v44, |v44|\n"
"v_mul_f32 v45, v45, |v45|\n"
"v_mul_f32 v46, v46, |v46|\n"
"v_mul_f32 v47, v47, |v47|\n"
"v_cvt_pk_bf16_f32 v96, v40, v41\n"
"v_cvt_pk_bf16_f32 v97, v42, v43\n"
"v_cvt_pk_bf16_f32 v98, v44, v45\n"
"v_cvt_pk_bf16_f32 v99, v46, v47\n"
"v_pk_max_i16 v96, v96, 0\n"
"v_pk_max_i16 v97, v97, 0\n"
"v_pk_max_i16 v98, v98, 0\n"
"v_pk_max_i16 v99, v99, 0\n"
"buffer_store_dwordx4 v[96:99], v2, s[40:43], s75 offen offset:1024 sc1\n"
"s_branch Lajoin_32_%=\n"
"Lqepi_60_%=:\n"
"v_rcp_f32 v6, v14\n"
"v_rcp_f32 v8, v15\n"
"v_add_u32 v10, s80, v2\n"
"v_mul_f32 v6, 0x3eb8aa3b, v6\n"
"v_mul_f32 v8, 0x3eb8aa3b, v8\n"
"s_nop 3\n"
"v_pk_mul_f32 v[16:17], v[16:17], v[6:7] op_sel_hi:[1,0]\n"
"v_pk_mul_f32 v[18:19], v[18:19], v[6:7] op_sel_hi:[1,0]\n"
"v_pk_mul_f32 v[20:21], v[20:21], v[6:7] op_sel_hi:[1,0]\n"
"v_pk_mul_f32 v[22:23], v[22:23], v[6:7] op_sel_hi:[1,0]\n"
"v_cvt_pk_bf16_f32 v84, v16, v17\n"
"v_cvt_pk_bf16_f32 v85, v18, v19\n"
"v_cvt_pk_bf16_f32 v86, v20, v21\n"
"v_cvt_pk_bf16_f32 v87, v22, v23\n"
"ds_write_b128 v10, v[84:87] offset:12288\n"
"v_pk_mul_f32 v[24:25], v[24:25], v[6:7] op_sel_hi:[1,0]\n"
"v_pk_mul_f32 v[26:27], v[26:27], v[6:7] op_sel_hi:[1,0]\n"
"v_pk_mul_f32 v[28:29], v[28:29], v[6:7] op_sel_hi:[1,0]\n"
"v_pk_mul_f32 v[30:31], v[30:31], v[6:7] op_sel_hi:[1,0]\n"
"v_cvt_pk_bf16_f32 v88, v24, v25\n"
"v_cvt_pk_bf16_f32 v89, v26, v27\n"
"v_cvt_pk_bf16_f32 v90, v28, v29\n"
"v_cvt_pk_bf16_f32 v91, v30, v31\n"
"ds_write_b128 v10, v[88:91] offset:13312\n"
"v_pk_mul_f32 v[32:33], v[32:33], v[8:9] op_sel_hi:[1,0]\n"
"v_pk_mul_f32 v[34:35], v[34:35], v[8:9] op_sel_hi:[1,0]\n"
"v_pk_mul_f32 v[36:37], v[36:37], v[8:9] op_sel_hi:[1,0]\n"
"v_pk_mul_f32 v[38:39], v[38:39], v[8:9] op_sel_hi:[1,0]\n"
"v_cvt_pk_bf16_f32 v92, v32, v33\n"
"v_cvt_pk_bf16_f32 v93, v34, v35\n"
"v_cvt_pk_bf16_f32 v94, v36, v37\n"
"v_cvt_pk_bf16_f32 v95, v38, v39\n"
"ds_write_b128 v10, v[92:95] offset:14336\n"
"v_pk_mul_f32 v[40:41], v[40:41], v[8:9] op_sel_hi:[1,0]\n"
"v_pk_mul_f32 v[42:43], v[42:43], v[8:9] op_sel_hi:[1,0]\n"
"v_pk_mul_f32 v[44:45], v[44:45], v[8:9] op_sel_hi:[1,0]\n"
"v_pk_mul_f32 v[46:47], v[46:47], v[8:9] op_sel_hi:[1,0]\n"
"v_cvt_pk_bf16_f32 v96, v40, v41\n"
"v_cvt_pk_bf16_f32 v97, v42, v43\n"
"v_cvt_pk_bf16_f32 v98, v44, v45\n"
"v_cvt_pk_bf16_f32 v99, v46, v47\n"
"ds_write_b128 v10, v[96:99] offset:15360\n"
"s_branch Lajoin_32_%=\n"
"Lanotile_33_%=:\n"
"s_mov_b32 s81, 2\n"
"Lajoin_32_%=:\n"
"s_cmp_eq_u32 s81, 1\n"
"s_cbranch_scc1 Lhave_61_%=\n"
"s_cmp_eq_u32 s81, 2\n"
"s_cbranch_scc1 Lslow_65_%=\n"
"s_cmp_ge_u32 s71, 2\n"
"s_cbranch_scc1 Lq0_67_%=\n"
"s_waitcnt vmcnt(4)\n"
"s_branch Lq0_67_%=x\n"
"Lq0_67_%=:\n"
"s_waitcnt vmcnt(0)\n"
"Lq0_67_%=x:\n"
"s_mov_b64 exec, 15\n"
"v_cmp_ne_u64 vcc, s[72:73], v[166:167]\n"
"s_mov_b64 exec, -1\n"
"s_cbranch_vccz Lfast_66_%=\n"
"Lslow_65_%=:\n"
"s_mov_b32 s74, 524288\n"
"Lspin_68_%=:\n"
"s_mov_b64 exec, 15\n"
"buffer_load_dwordx2 v[166:167], v231, s[40:43], s82 offen sc1\n"
"s_mov_b64 exec, -1\n"
"Ltst_70_%=:\n"
"s_waitcnt vmcnt(0)\n"
"s_mov_b64 exec, 15\n"
"v_cmp_ne_u64 vcc, s[72:73], v[166:167]\n"
"s_mov_b64 exec, -1\n"
"s_cbranch_vccz Lgot_69_%=\n"
"s_sleep 2\n"
"s_sub_u32 s74, s74, 1\n"
"s_cmp_lg_u32 s74, 0\n"
"s_cbranch_scc1 Lspin_68_%=\n"
"Lgot_69_%=:\n"
"Lfast_66_%=:\n"
"s_lshl_b32 s75, s71, 13\n"
"s_add_u32 m0, s75, 138240\n"
"s_lshr_b32 s75, s75, 4\n"
"s_add_u32 s75, s75, 7901184\n"
"s_mov_b32 exec_hi, 0\n"
"buffer_load_dwordx4 v2, s[40:43], s75 offen offset:0 sc1 lds\n"
"s_mov_b32 exec_hi, -1\n"
"s_add_u32 m0, s80, 0\n"
"s_add_u32 s75, s83, 0\n"
"buffer_load_dwordx4 v2, s[40:43], s75 offen offset:0 sc1 lds\n"
"buffer_load_dwordx4 v2, s[40:43], s75 offen offset:1024 sc1 lds\n"
"s_add_u32 m0, s80, 0\n"
"s_add_u32 s75, s83, 0\n"
"buffer_load_dwordx4 v2, s[40:43], s75 offen offset:2048 sc1 lds\n"
"buffer_load_dwordx4 v2, s[40:43], s75 offen offset:3072 sc1 lds\n"
"Lhave_61_%=:\n"
"s_mov_b32 s60, s24\n"
"s_and_b32 s61, s25, 0xffff\n"
"s_mov_b32 s62, 1024\n"
"s_mov_b32 s63, 0x20000\n"
"s_cmp_ge_u32 s71, 2\n"
"s_cbranch_scc1 Lw8_62_%=\n"
"s_cmp_lg_u32 s81, 1\n"
"s_cbranch_scc1 Lw8_62_%=\n"
"s_waitcnt vmcnt(4)\n"
"s_branch Lwd_64_%=\n"
"Lw8_62_%=:\n"
"s_waitcnt vmcnt(0)\n"
"Lwd_64_%=:\n"
"s_waitcnt lgkmcnt(0)\n"
"s_barrier\n"
"ds_read_b128 v[132:135], v240\n"
"ds_read_b128 v[136:139], v240 offset:2048\n"
"ds_read_b128 v[80:83], v225\n"
"ds_read_b128 v[84:87], v225 offset:16384\n"
"ds_read_b128 v[140:143], v236 offset:0\n"
"ds_read_b128 v[144:147], v236 offset:16384\n"
"s_mov_b64 exec, s[90:91]\n"
"ds_read_b128 v[148:151], v238 offset:0\n"
"ds_read_b128 v[152:155], v238 offset:512\n"
"s_mov_b64 exec, -1\n"
"s_waitcnt lgkmcnt(4)\n"
"v_mfma_f32_32x32x16_bf16 v[16:31], v[80:83], v[132:135], 0\n"
"v_mfma_f32_32x32x16_bf16 v[48:63], v[80:83], v[136:139], 0\n"
"v_mfma_f32_32x32x16_bf16 v[32:47], v[84:87], v[132:135], 0\n"
"v_mfma_f32_32x32x16_bf16 v[64:79], v[84:87], v[136:139], 0\n"
"s_nop 8\n"
"v_max_f32 v4, v16, v17\n"
"v_max_f32 v6, v48, v49\n"
"v_max3_f32 v4, v4, v18, v19\n"
"v_max3_f32 v6, v6, v50, v51\n"
"v_max3_f32 v4, v4, v20, v21\n"
"v_max3_f32 v6, v6, v52, v53\n"
"v_max3_f32 v4, v4, v22, v23\n"
"v_max3_f32 v6, v6, v54, v55\n"
"v_max3_f32 v4, v4, v24, v25\n"
"v_max3_f32 v6, v6, v56, v57\n"
"v_max3_f32 v4, v4, v26, v27\n"
"v_max3_f32 v6, v6, v58, v59\n"
"v_max3_f32 v4, v4, v28, v29\n"
"v_max3_f32 v6, v6, v60, v61\n"
"v_max3_f32 v4, v4, v30, v31\n"
"v_max3_f32 v6, v6, v62, v63\n"
"v_max3_f32 v4, v4, v32, v33\n"
"v_max3_f32 v6, v6, v64, v65\n"
"v_max3_f32 v4, v4, v34, v35\n"
"v_max3_f32 v6, v6, v66, v67\n"
"v_max3_f32 v4, v4, v36, v37\n"
"v_max3_f32 v6, v6, v68, v69\n"
"v_max3_f32 v4, v4, v38, v39\n"
"v_max3_f32 v6, v6, v70, v71\n"
"v_max3_f32 v4, v4, v40, v41\n"
"v_max3_f32 v6, v6, v72, v73\n"
"v_max3_f32 v4, v4, v42, v43\n"
"v_max3_f32 v6, v6, v74, v75\n"
"v_max3_f32 v4, v4, v44, v45\n"
"v_max3_f32 v6, v6, v76, v77\n"
"v_max3_f32 v4, v4, v46, v47\n"
"v_max3_f32 v6, v6, v78, v79\n"
"v_mov_b32 v5, v4\n"
"v_mov_b32 v7, v6\n"
"s_nop 0\n"
"v_permlane32_swap_b32 v4, v5\n"
"v_permlane32_swap_b32 v6, v7\n"
"v_max_f32 v4, v4, v5\n"
"v_max_f32 v6, v6, v7\n"
"v_sub_f32 v48, 0xc2cc0000, v4\n"
"v_sub_f32 v64, 0xc2cc0000, v6\n"
"s_waitcnt lgkmcnt(2)\n"
"v_mov_b32 v49, v48\n"
"v_pk_mov_b32 v[50:51], v[48:49], v[48:49]\n"
"v_pk_mov_b32 v[52:53], v[48:49], v[48:49]\n"
"v_pk_mov_b32 v[54:55], v[48:49], v[48:49]\n"
"v_pk_mov_b32 v[56:57], v[48:49], v[48:49]\n"
"v_pk_mov_b32 v[58:59], v[48:49], v[48:49]\n"
"v_pk_mov_b32 v[60:61], v[48:49], v[48:49]\n"
"v_pk_mov_b32 v[62:63], v[48:49], v[48:49]\n"
"s_nop 1\n"
"v_mfma_f32_32x32x16_bf16 v[16:31], v[140:143], v[132:135], v[48:63]\n"
"v_mov_b32 v65, v64\n"
"v_pk_mov_b32 v[66:67], v[64:65], v[64:65]\n"
"v_pk_mov_b32 v[68:69], v[64:65], v[64:65]\n"
"v_pk_mov_b32 v[70:71], v[64:65], v[64:65]\n"
"v_pk_mov_b32 v[72:73], v[64:65], v[64:65]\n"
"v_pk_mov_b32 v[74:75], v[64:65], v[64:65]\n"
"v_pk_mov_b32 v[76:77], v[64:65], v[64:65]\n"
"v_pk_mov_b32 v[78:79], v[64:65], v[64:65]\n"
"ds_write2st64_b32 v241, v48, v64 offset0:0 offset1:1\n"
"s_branch Lattn_31_%=\n"
"Lbstart_30_%=:\n"
"s_setprio 0\n"
"v_lshrrev_b32 v6, 3, v1\n"
"v_and_b32 v5, 7, v1\n"
"v_and_b32 v7, 31, v1\n"
"v_lshrrev_b32 v8, 5, v1\n"
"v_lshrrev_b32 v9, 4, v1\n"
"v_bfe_u32 v10, v1, 2, 2\n"
"v_and_b32 v11, 3, v1\n"
"s_mul_i32 s75, s69, 6\n"
"s_add_u32 s75, s75, s71\n"
"s_sub_u32 s75, s75, 3\n"
"s_cmp_eq_u32 s69, 0\n"
"s_cselect_b32 s85, 0, s75\n"
"s_lshl_b32 s61, s85, 6\n"
"s_movk_i32 s78, 2752\n"
"s_mov_b32 s36, s18\n"
"s_and_b32 s37, s19, 0xffff\n"
"s_mov_b32 s38, 1409024\n"
"s_mov_b32 s39, 0x20000\n"
"s_lshl_b32 s83, s71, 14\n"
"s_lshl_b32 s80, s71, 13\n"
"s_add_u32 s80, s80, 131072\n"
"v_lshrrev_b32 v226, 2, v1\n"
"v_mul_u32_u24 v226, s78, v226\n"
"v_lshl_add_u32 v226, v11, 4, v226\n"
"s_lshl_b32 s78, s78, 4\n"
"s_mul_i32 s75, s60, s78\n"
"s_lshl_b32 s75, s75, 3\n"
"s_add_u32 s79, s61, s75\n"
"s_lshl_b32 s75, s60, 13\n"
"v_lshl_add_u32 v18, v8, 9, s75\n"
"v_xor_b32 v19, v8, v7\n"
"v_lshl_add_u32 v232, v19, 4, v18\n"
"v_xor_b32 v20, 2, v19\n"
"v_lshl_add_u32 v20, v20, 4, v18\n"
"v_add_u32 v233, 1024, v20\n"
"v_xor_b32 v20, 4, v19\n"
"v_lshl_add_u32 v20, v20, 4, v18\n"
"v_add_u32 v234, 2048, v20\n"
"v_xor_b32 v20, 6, v19\n"
"v_lshl_add_u32 v20, v20, 4, v18\n"
"v_add_u32 v235, 3072, v20\n"
"v_lshl_add_u32 v18, v8, 3, v10\n"
"v_lshlrev_b32 v18, 6, v18\n"
"v_and_b32 v19, 1, v9\n"
"v_lshl_add_u32 v18, v19, 5, v18\n"
"v_lshl_add_u32 v18, v11, 3, v18\n"
"v_add_u32 v228, s80, v18\n"
"s_mov_b32 s56, s20\n"
"s_and_b32 s57, s21, 0xffff\n"
"s_mov_b32 s58, 2752\n"
"s_mov_b32 s59, 0x20000\n"
"s_lshl_b32 s75, s85, 5\n"
"v_add_lshl_u32 v227, v7, s75, 1\n"
"s_mul_i32 s74, s70, 172\n"
"s_lshl_b32 s75, s85, 1\n"
"s_add_u32 s75, s74, s75\n"
"s_lshl_b32 s75, s75, 10\n"
"s_add_u32 s84, s75, 131072\n"
"s_lshl_b32 s75, s68, 2\n"
"v_add_lshl_u32 v231, v11, s75, 7\n"
"s_mov_b32 s82, 7864320\n"
"s_lshl_b32 s75, s70, 6\n"
"s_lshl_b32 s76, s69, 2\n"
"s_add_u32 s75, s75, s76\n"
"s_add_u32 s75, s75, s71\n"
"s_lshl_b32 s75, s75, 10\n"
"s_add_u32 s86, s75, 5767168\n"
"v_and_b32 v4, 31, v1\n"
"v_lshrrev_b32 v5, 5, v1\n"
"v_add_u32 v236, 0, v2\n"
"v_add_u32 v237, 32768, v2\n"
"v_lshlrev_b32 v5, 8, v5\n"
"v_lshl_add_u32 v5, v4, 4, v5\n"
"v_add_u32 v238, 1024, v5\n"
"v_add_u32 v239, 33792, v5\n"
"s_lshr_b32 s75, s71, 1\n"
"s_lshl_b32 s75, s75, 14\n"
"s_and_b32 s76, s71, 1\n"
"s_lshl_b32 s76, s76, 10\n"
"s_add_u32 s75, s75, s76\n"
"s_add_u32 s75, s75, 110592\n"
"v_add_u32 v240, s75, v2\n"
"s_lshl_b32 s75, s71, 13\n"
"s_add_u32 s75, s75, 131072\n"
"v_add_u32 v242, s75, v2\n"
"v_lshl_add_u32 v5, v1, 2, s75\n"
"v_add_u32 v241, 7680, v5\n"
"v_add_u32 v229, 4096, v5\n"
"v_mov_b32 v204, s72\n"
"v_mov_b32 v205, s73\n"
"v_mov_b32 v206, 0\n"
"s_lshl_b32 s76, s70, 3\n"
"s_add_u32 s76, s76, s69\n"
"s_lshl_b32 s76, s76, 4\n"
"s_add_u32 s76, s76, s68\n"
"s_lshl_b32 s76, s76, 3\n"
"s_add_u32 s85, s76, 7868416\n"
"s_mov_b32 s90, 0xffff\n"
"s_mov_b32 s91, 0xffff\n"
"v_mov_b32 v164, 0xdddd\n"
"v_and_b32 v4, 27, v4\n"
"v_cmp_eq_u32 s[30:31], 16, v4\n"
"v_mov_b32 v5, 0x3f803f80\n"
"s_nop 0\n"
"v_cndmask_b32 v4, 0, v5, s[30:31]\n"
"v_mov_b32 v5, v4\n"
"v_pk_mov_b32 v[148:149], v[4:5], v[4:5]\n"
"v_pk_mov_b32 v[150:151], v[4:5], v[4:5]\n"
"v_pk_mov_b32 v[152:153], v[4:5], v[4:5]\n"
"v_pk_mov_b32 v[154:155], v[4:5], v[4:5]\n"
"v_pk_mov_b32 v[156:157], v[4:5], v[4:5]\n"
"v_pk_mov_b32 v[158:159], v[4:5], v[4:5]\n"
"v_pk_mov_b32 v[160:161], v[4:5], v[4:5]\n"
"v_pk_mov_b32 v[162:163], v[4:5], v[4:5]\n"
"v_mov_b32 v4, 0\n"
"v_mov_b32 v5, 0\n"
"v_pk_mov_b32 v[100:101], v[4:5], v[4:5]\n"
"v_pk_mov_b32 v[102:103], v[4:5], v[4:5]\n"
"v_pk_mov_b32 v[104:105], v[4:5], v[4:5]\n"
"v_pk_mov_b32 v[106:107], v[4:5], v[4:5]\n"
"v_pk_mov_b32 v[108:109], v[4:5], v[4:5]\n"
"v_pk_mov_b32 v[110:111], v[4:5], v[4:5]\n"
"v_pk_mov_b32 v[112:113], v[4:5], v[4:5]\n"
"v_pk_mov_b32 v[114:115], v[4:5], v[4:5]\n"
"v_pk_mov_b32 v[116:117], v[4:5], v[4:5]\n"
"v_pk_mov_b32 v[118:119], v[4:5], v[4:5]\n"
"v_pk_mov_b32 v[120:121], v[4:5], v[4:5]\n"
"v_pk_mov_b32 v[122:123], v[4:5], v[4:5]\n"
"v_pk_mov_b32 v[124:125], v[4:5], v[4:5]\n"
"v_pk_mov_b32 v[126:127], v[4:5], v[4:5]\n"
"v_pk_mov_b32 v[128:129], v[4:5], v[4:5]\n"
"v_pk_mov_b32 v[130:131], v[4:5], v[4:5]\n"
"s_mov_b32 s44, s22\n"
"s_and_b32 s45, s23, 0xffff\n"
"s_mov_b32 s46, 1409024\n"
"s_mov_b32 s47, 0x20000\n"
"s_mov_b32 s48, s16\n"
"s_and_b32 s49, s17, 0xffff\n"
"s_mov_b32 s50, 524288\n"
"s_mov_b32 s51, 0x20000\n"
"s_mov_b32 s52, s26\n"
"s_and_b32 s53, s27, 0xffff\n"
"s_mov_b32 s54, 2097152\n"
"s_mov_b32 s55, 0x20000\n"
"s_lshl_b32 s75, s69, 7\n"
"s_lshl_b32 s76, s71, 5\n"
"s_add_u32 s97, s75, s76\n"
"s_lshl_b32 s76, s70, 16\n"
"s_add_u32 s87, s97, s76\n"
"v_lshrrev_b32 v4, 5, v1\n"
"v_lshlrev_b32 v248, 3, v4\n"
"v_lshl_add_u32 v247, v7, 10, v248\n"
"v_lshl_add_u32 v249, v4, 3, v247\n"
"s_mul_i32 s76, s71, 14336\n"
"s_add_u32 s76, s76, 0\n"
"v_add_u32 v250, s76, v2\n"
"v_mov_b32 v20, -1\n"
"v_mov_b32 v21, -1\n"
"v_mov_b32 v22, -1\n"
"v_mov_b32 v23, -1\n"
"s_add_u32 s74, s86, 32768\n"
"buffer_store_dwordx4 v[20:23], v2, s[40:43], s86 offen sc1\n"
"buffer_store_dwordx4 v[20:23], v2, s[40:43], s74 offen sc1\n"
"buffer_load_ushort v227, v227, s[56:59], 0 offen\n"
"s_waitcnt lgkmcnt(0)\n"
"s_barrier\n"
"ds_read_b128 v[132:135], v240\n"
"ds_read_b128 v[136:139], v240 offset:2048\n"
"s_cmp_eq_u32 s71, 0\n"
"s_cselect_b32 s75, 1, s69\n"
"s_cmp_eq_u32 s75, 0\n"
"s_cbranch_scc1 Lbnotile_71_%=\n"
"s_setprio 0\n"
"s_add_u32 m0, s80, 0\n"
"s_nop 0\n"
"buffer_load_dwordx4 v226, s[36:39], s79 offen lds\n"
"s_add_u32 s79, s79, s78\n"
"s_add_u32 m0, s80, 1024\n"
"s_nop 0\n"
"buffer_load_dwordx4 v226, s[36:39], s79 offen lds\n"
"s_add_u32 s79, s79, s78\n"
"s_add_u32 m0, s80, 2048\n"
"s_nop 0\n"
"buffer_load_dwordx4 v226, s[36:39], s79 offen lds\n"
"s_add_u32 s79, s79, s78\n"
"s_add_u32 m0, s80, 3072\n"
"s_nop 0\n"
"buffer_load_dwordx4 v226, s[36:39], s79 offen lds\n"
"s_add_u32 s79, s79, s78\n"
"s_add_u32 m0, s80, 4096\n"
"s_nop 0\n"
"buffer_load_dwordx4 v226, s[36:39], s79 offen lds\n"
"s_add_u32 s79, s79, s78\n"
"s_add_u32 m0, s80, 5120\n"
"s_nop 0\n"
"buffer_load_dwordx4 v226, s[36:39], s79 offen lds\n"
"s_add_u32 s79, s79, s78\n"
"s_add_u32 m0, s80, 6144\n"
"s_nop 0\n"
"buffer_load_dwordx4 v226, s[36:39], s79 offen lds\n"
"s_add_u32 s79, s79, s78\n"
"s_add_u32 m0, s83, 69632\n"
"s_add_u32 s75, s83, 4096\n"
"buffer_load_dwordx4 v2, s[40:43], s75 offen offset:0 sc1 lds\n"
"buffer_load_dwordx4 v2, s[40:43], s75 offen offset:1024 sc1 lds\n"
"buffer_load_dwordx4 v2, s[40:43], s75 offen offset:2048 sc1 lds\n"
"buffer_load_dwordx4 v2, s[40:43], s75 offen offset:3072 sc1 lds\n"
"s_add_u32 m0, s83, 73728\n"
"s_add_u32 s75, s83, 8192\n"
"buffer_load_dwordx4 v2, s[40:43], s75 offen offset:0 sc1 lds\n"
"buffer_load_dwordx4 v2, s[40:43], s75 offen offset:1024 sc1 lds\n"
"buffer_load_dwordx4 v2, s[40:43], s75 offen offset:2048 sc1 lds\n"
"buffer_load_dwordx4 v2, s[40:43], s75 offen offset:3072 sc1 lds\n"
"s_waitcnt vmcnt(15)\n"
"s_waitcnt lgkmcnt(0)\n"
"v_cmp_gt_u32 s[30:31], 32, v1\n"
"v_mov_b32 v56, v227\n"
"v_mov_b32 v57, 0\n"
"v_pk_mov_b32 v[58:59], v[56:57], v[56:57] op_sel:[1,1]\n"
"v_lshrrev_b32 v6, 16, v14\n"
"v_cndmask_b32 v76, 0, v6, s[30:31]\n"
"v_mov_b32 v77, 0\n"
"v_pk_mov_b32 v[78:79], v[58:59], v[58:59]\n"
"v_lshrrev_b32 v6, 16, v15\n"
"v_cndmask_b32 v80, 0, v6, s[30:31]\n"
"v_mov_b32 v81, 0\n"
"v_pk_mov_b32 v[82:83], v[58:59], v[58:59]\n"
"v_mfma_f32_32x32x16_bf16 v[16:31], v[56:59], v[76:79], 0\n"
"s_nop 0\n"
"v_mfma_f32_32x32x16_bf16 v[32:47], v[56:59], v[80:83], 0\n"
"s_waitcnt vmcnt(14)\n"
"ds_read_b64_tr_b16 v[48:49], v228 offset:0\n"
"ds_read_b64_tr_b16 v[50:51], v228 offset:256\n"
"ds_read_b128 v[60:63], v232 offset:0\n"
"ds_read_b128 v[64:67], v232 offset:32768\n"
"s_waitcnt vmcnt(13)\n"
"ds_read_b64_tr_b16 v[52:53], v228 offset:1024\n"
"ds_read_b64_tr_b16 v[54:55], v228 offset:1280\n"
"ds_read_b128 v[68:71], v233 offset:0\n"
"ds_read_b128 v[72:75], v233 offset:32768\n"
"s_waitcnt vmcnt(12) lgkmcnt(4)\n"
"s_add_u32 m0, s80, 0\n"
"v_mfma_f32_32x32x16_bf16 v[16:31], v[48:51], v[60:63], v[16:31]\n"
"ds_read_b64_tr_b16 v[56:57], v228 offset:2048\n"
"ds_read_b64_tr_b16 v[58:59], v228 offset:2304\n"
"ds_read_b128 v[76:79], v234 offset:0\n"
"ds_read_b128 v[80:83], v234 offset:32768\n"
"buffer_load_dwordx4 v226, s[36:39], s79 offen lds\n"
"s_add_u32 s79, s79, s78\n"
"s_cmp_eq_u32 s60, 3\n"
"s_cselect_b32 s79, s61, s79\n"
"v_mfma_f32_32x32x16_bf16 v[32:47], v[48:51], v[64:67], v[32:47]\n"
"s_waitcnt vmcnt(12) lgkmcnt(4)\n"
"s_add_u32 m0, s80, 1024\n"
"v_mfma_f32_32x32x16_bf16 v[16:31], v[52:55], v[68:71], v[16:31]\n"
"ds_read_b64_tr_b16 v[48:49], v228 offset:3072\n"
"ds_read_b64_tr_b16 v[50:51], v228 offset:3328\n"
"ds_read_b128 v[60:63], v235 offset:0\n"
"ds_read_b128 v[64:67], v235 offset:32768\n"
"buffer_load_dwordx4 v226, s[36:39], s79 offen lds\n"
"s_add_u32 s79, s79, s78\n"
"v_mfma_f32_32x32x16_bf16 v[32:47], v[52:55], v[72:75], v[32:47]\n"
"s_waitcnt vmcnt(12) lgkmcnt(4)\n"
"s_add_u32 m0, s80, 2048\n"
"v_mfma_f32_32x32x16_bf16 v[16:31], v[56:59], v[76:79], v[16:31]\n"
"ds_read_b64_tr_b16 v[52:53], v228 offset:4096\n"
"ds_read_b64_tr_b16 v[54:55], v228 offset:4352\n"
"ds_read_b128 v[68:71], v232 offset:4096\n"
"ds_read_b128 v[72:75], v232 offset:36864\n"
"buffer_load_dwordx4 v226, s[36:39], s79 offen lds\n"
"s_add_u32 s79, s79, s78\n"
"v_mfma_f32_32x32x16_bf16 v[32:47], v[56:59], v[80:83], v[32:47]\n"
"s_waitcnt vmcnt(12) lgkmcnt(4)\n"
"s_add_u32 m0, s80, 3072\n"
"v_mfma_f32_32x32x16_bf16 v[16:31], v[48:51], v[60:63], v[16:31]\n"
"ds_read_b64_tr_b16 v[56:57], v228 offset:5120\n"
"ds_read_b64_tr_b16 v[58:59], v228 offset:5376\n"
"ds_read_b128 v[76:79], v233 offset:4096\n"
"ds_read_b128 v[80:83], v233 offset:36864\n"
"buffer_load_dwordx4 v226, s[36:39], s79 offen lds\n"
"s_add_u32 s79, s79, s78\n"
"v_mfma_f32_32x32x16_bf16 v[32:47], v[48:51], v[64:67], v[32:47]\n"
"s_waitcnt vmcnt(4)\n"
"s_barrier\n"
"s_add_u32 m0, s83, 77824\n"
"s_add_u32 s75, s83, 12288\n"
"buffer_load_dwordx4 v2, s[40:43], s75 offen offset:0 sc1 lds\n"
"buffer_load_dwordx4 v2, s[40:43], s75 offen offset:1024 sc1 lds\n"
"buffer_load_dwordx4 v2, s[40:43], s75 offen offset:2048 sc1 lds\n"
"buffer_load_dwordx4 v2, s[40:43], s75 offen offset:3072 sc1 lds\n"
"s_waitcnt vmcnt(16) lgkmcnt(4)\n"
"s_add_u32 m0, s80, 4096\n"
"v_mfma_f32_32x32x16_bf16 v[16:31], v[52:55], v[68:71], v[16:31]\n"
"ds_read_b64_tr_b16 v[48:49], v228 offset:6144\n"
"ds_read_b64_tr_b16 v[50:51], v228 offset:6400\n"
"ds_read_b128 v[60:63], v234 offset:4096\n"
"ds_read_b128 v[64:67], v234 offset:36864\n"
"buffer_load_dwordx4 v226, s[36:39], s79 offen lds\n"
"s_add_u32 s79, s79, s78\n"
"v_mfma_f32_32x32x16_bf16 v[32:47], v[52:55], v[72:75], v[32:47]\n"
"s_waitcnt vmcnt(8) lgkmcnt(4)\n"
"s_add_u32 m0, s80, 5120\n"
"v_mfma_f32_32x32x16_bf16 v[16:31], v[56:59], v[76:79], v[16:31]\n"
"ds_read_b64_tr_b16 v[52:53], v228 offset:0\n"
"ds_read_b64_tr_b16 v[54:55], v228 offset:256\n"
"ds_read_b128 v[68:71], v235 offset:4096\n"
"ds_read_b128 v[72:75], v235 offset:36864\n"
"buffer_load_dwordx4 v226, s[36:39], s79 offen lds\n"
"s_add_u32 s79, s79, s78\n"
"v_mfma_f32_32x32x16_bf16 v[32:47], v[56:59], v[80:83], v[32:47]\n"
"s_waitcnt vmcnt(8) lgkmcnt(4)\n"
"s_add_u32 m0, s80, 6144\n"
"v_mfma_f32_32x32x16_bf16 v[16:31], v[48:51], v[60:63], v[16:31]\n"
"ds_read_b64_tr_b16 v[56:57], v228 offset:1024\n"
"ds_read_b64_tr_b16 v[58:59], v228 offset:1280\n"
"s_cmp_eq_u32 s60, 3\n"
"s_mov_b32 s75, 8192\n"
"s_cselect_b32 s75, -24576, s75\n"
"v_add_u32 v232, s75, v232\n"
"v_add_u32 v233, s75, v233\n"
"v_add_u32 v234, s75, v234\n"
"v_add_u32 v235, s75, v235\n"
"ds_read_b128 v[76:79], v232 offset:0\n"
"ds_read_b128 v[80:83], v232 offset:32768\n"
"buffer_load_dwordx4 v226, s[36:39], s79 offen lds\n"
"s_add_u32 s79, s79, s78\n"
"v_mfma_f32_32x32x16_bf16 v[32:47], v[48:51], v[64:67], v[32:47]\n"
"s_waitcnt vmcnt(8) lgkmcnt(4)\n"
"s_add_u32 m0, s80, 0\n"
"v_mfma_f32_32x32x16_bf16 v[16:31], v[52:55], v[68:71], v[16:31]\n"
"ds_read_b64_tr_b16 v[48:49], v228 offset:2048\n"
"ds_read_b64_tr_b16 v[50:51], v228 offset:2304\n"
"ds_read_b128 v[60:63], v233 offset:0\n"
"ds_read_b128 v[64:67], v233 offset:32768\n"
"buffer_load_dwordx4 v226, s[36:39], s79 offen lds\n"
"s_add_u32 s79, s79, s78\n"
"v_mfma_f32_32x32x16_bf16 v[32:47], v[52:55], v[72:75], v[32:47]\n"
"s_waitcnt vmcnt(8) lgkmcnt(4)\n"
"s_add_u32 m0, s80, 1024\n"
"v_mfma_f32_32x32x16_bf16 v[16:31], v[56:59], v[76:79], v[16:31]\n"
"ds_read_b64_tr_b16 v[52:53], v228 offset:3072\n"
"ds_read_b64_tr_b16 v[54:55], v228 offset:3328\n"
"ds_read_b128 v[68:71], v234 offset:0\n"
"ds_read_b128 v[72:75], v234 offset:32768\n"
"buffer_load_dwordx4 v226, s[36:39], s79 offen lds\n"
"s_add_u32 s79, s79, s78\n"
"s_cmp_eq_u32 s60, 2\n"
"s_cselect_b32 s79, s61, s79\n"
"v_mfma_f32_32x32x16_bf16 v[32:47], v[56:59], v[80:83], v[32:47]\n"
"s_waitcnt vmcnt(4) lgkmcnt(4)\n"
"s_add_u32 m0, s80, 2048\n"
"v_mfma_f32_32x32x16_bf16 v[16:31], v[48:51], v[60:63], v[16:31]\n"
"ds_read_b64_tr_b16 v[56:57], v228 offset:4096\n"
"ds_read_b64_tr_b16 v[58:59], v228 offset:4352\n"
"ds_read_b128 v[76:79], v235 offset:0\n"
"ds_read_b128 v[80:83], v235 offset:32768\n"
"buffer_load_dwordx4 v226, s[36:39], s79 offen lds\n"
"s_add_u32 s79, s79, s78\n"
"v_mfma_f32_32x32x16_bf16 v[32:47], v[48:51], v[64:67], v[32:47]\n"
"s_waitcnt vmcnt(4) lgkmcnt(4)\n"
"s_add_u32 m0, s80, 3072\n"
"v_mfma_f32_32x32x16_bf16 v[16:31], v[52:55], v[68:71], v[16:31]\n"
"ds_read_b64_tr_b16 v[48:49], v228 offset:5120\n"
"ds_read_b64_tr_b16 v[50:51], v228 offset:5376\n"
"ds_read_b128 v[60:63], v232 offset:4096\n"
"ds_read_b128 v[64:67], v232 offset:36864\n"
"buffer_load_dwordx4 v226, s[36:39], s79 offen lds\n"
"s_add_u32 s79, s79, s78\n"
"v_mfma_f32_32x32x16_bf16 v[32:47], v[52:55], v[72:75], v[32:47]\n"
"s_waitcnt vmcnt(4) lgkmcnt(4)\n"
"s_add_u32 m0, s80, 4096\n"
"v_mfma_f32_32x32x16_bf16 v[16:31], v[56:59], v[76:79], v[16:31]\n"
"ds_read_b64_tr_b16 v[52:53], v228 offset:6144\n"
"ds_read_b64_tr_b16 v[54:55], v228 offset:6400\n"
"ds_read_b128 v[68:71], v233 offset:4096\n"
"ds_read_b128 v[72:75], v233 offset:36864\n"
"buffer_load_dwordx4 v226, s[36:39], s79 offen lds\n"
"s_add_u32 s79, s79, s78\n"
"v_mfma_f32_32x32x16_bf16 v[32:47], v[56:59], v[80:83], v[32:47]\n"
"s_waitcnt vmcnt(4) lgkmcnt(4)\n"
"s_add_u32 m0, s80, 5120\n"
"v_mfma_f32_32x32x16_bf16 v[16:31], v[48:51], v[60:63], v[16:31]\n"
"ds_read_b64_tr_b16 v[56:57], v228 offset:0\n"
"ds_read_b64_tr_b16 v[58:59], v228 offset:256\n"
"ds_read_b128 v[76:79], v234 offset:4096\n"
"ds_read_b128 v[80:83], v234 offset:36864\n"
"buffer_load_dwordx4 v226, s[36:39], s79 offen lds\n"
"s_add_u32 s79, s79, s78\n"
"v_mfma_f32_32x32x16_bf16 v[32:47], v[48:51], v[64:67], v[32:47]\n"
"s_waitcnt vmcnt(4) lgkmcnt(4)\n"
"s_add_u32 m0, s80, 6144\n"
"v_mfma_f32_32x32x16_bf16 v[16:31], v[52:55], v[68:71], v[16:31]\n"
"ds_read_b64_tr_b16 v[48:49], v228 offset:1024\n"
"ds_read_b64_tr_b16 v[50:51], v228 offset:1280\n"
"ds_read_b128 v[60:63], v235 offset:4096\n"
"ds_read_b128 v[64:67], v235 offset:36864\n"
"buffer_load_dwordx4 v226, s[36:39], s79 offen lds\n"
"s_add_u32 s79, s79, s78\n"
"v_mfma_f32_32x32x16_bf16 v[32:47], v[52:55], v[72:75], v[32:47]\n"
"s_waitcnt vmcnt(4) lgkmcnt(4)\n"
"s_add_u32 m0, s80, 0\n"
"v_mfma_f32_32x32x16_bf16 v[16:31], v[56:59], v[76:79], v[16:31]\n"
"ds_read_b64_tr_b16 v[52:53], v228 offset:2048\n"
"ds_read_b64_tr_b16 v[54:55], v228 offset:2304\n"
"s_cmp_eq_u32 s60, 2\n"
"s_mov_b32 s75, 8192\n"
"s_cselect_b32 s75, -24576, s75\n"
"v_add_u32 v232, s75, v232\n"
"v_add_u32 v233, s75, v233\n"
"v_add_u32 v234, s75, v234\n"
"v_add_u32 v235, s75, v235\n"
"ds_read_b128 v[68:71], v232 offset:0\n"
"ds_read_b128 v[72:75], v232 offset:32768\n"
"buffer_load_dwordx4 v226, s[36:39], s79 offen lds\n"
"s_add_u32 s79, s79, s78\n"
"v_mfma_f32_32x32x16_bf16 v[32:47], v[56:59], v[80:83], v[32:47]\n"
"s_waitcnt vmcnt(4) lgkmcnt(4)\n"
"s_add_u32 m0, s80, 1024\n"
"v_mfma_f32_32x32x16_bf16 v[16:31], v[48:51], v[60:63], v[16:31]\n"
"ds_read_b64_tr_b16 v[56:57], v228 offset:3072\n"
"ds_read_b64_tr_b16 v[58:59], v228 offset:3328\n"
"ds_read_b128 v[76:79], v233 offset:0\n"
"ds_read_b128 v[80:83], v233 offset:32768\n"
"buffer_load_dwordx4 v226, s[36:39], s79 offen lds\n"
"s_add_u32 s79, s79, s78\n"
"v_mfma_f32_32x32x16_bf16 v[32:47], v[48:51], v[64:67], v[32:47]\n"
"s_waitcnt vmcnt(4) lgkmcnt(4)\n"
"s_add_u32 m0, s80, 2048\n"
"v_mfma_f32_32x32x16_bf16 v[16:31], v[52:55], v[68:71], v[16:31]\n"
"ds_read_b64_tr_b16 v[48:49], v228 offset:4096\n"
"ds_read_b64_tr_b16 v[50:51], v228 offset:4352\n"
"ds_read_b128 v[60:63], v234 offset:0\n"
"ds_read_b128 v[64:67], v234 offset:32768\n"
"buffer_load_dwordx4 v226, s[36:39], s79 offen lds\n"
"s_add_u32 s79, s79, s78\n"
"s_cmp_eq_u32 s60, 1\n"
"s_cselect_b32 s79, s61, s79\n"
"v_mfma_f32_32x32x16_bf16 v[32:47], v[52:55], v[72:75], v[32:47]\n"
"s_waitcnt vmcnt(4) lgkmcnt(4)\n"
"s_add_u32 m0, s80, 3072\n"
"v_mfma_f32_32x32x16_bf16 v[16:31], v[56:59], v[76:79], v[16:31]\n"
"ds_read_b64_tr_b16 v[52:53], v228 offset:5120\n"
"ds_read_b64_tr_b16 v[54:55], v228 offset:5376\n"
"ds_read_b128 v[68:71], v235 offset:0\n"
"ds_read_b128 v[72:75], v235 offset:32768\n"
"buffer_load_dwordx4 v226, s[36:39], s79 offen lds\n"
"s_add_u32 s79, s79, s78\n"
"v_mfma_f32_32x32x16_bf16 v[32:47], v[56:59], v[80:83], v[32:47]\n"
"s_waitcnt vmcnt(4) lgkmcnt(4)\n"
"s_add_u32 m0, s80, 4096\n"
"v_mfma_f32_32x32x16_bf16 v[16:31], v[48:51], v[60:63], v[16:31]\n"
"ds_read_b64_tr_b16 v[56:57], v228 offset:6144\n"
"ds_read_b64_tr_b16 v[58:59], v228 offset:6400\n"
"ds_read_b128 v[76:79], v232 offset:4096\n"
"ds_read_b128 v[80:83], v232 offset:36864\n"
"buffer_load_dwordx4 v226, s[36:39], s79 offen lds\n"
"s_add_u32 s79, s79, s78\n"
"v_mfma_f32_32x32x16_bf16 v[32:47], v[48:51], v[64:67], v[32:47]\n"
"s_waitcnt vmcnt(4) lgkmcnt(4)\n"
"s_add_u32 m0, s80, 5120\n"
"v_mfma_f32_32x32x16_bf16 v[16:31], v[52:55], v[68:71], v[16:31]\n"
"ds_read_b64_tr_b16 v[48:49], v228 offset:0\n"
"ds_read_b64_tr_b16 v[50:51], v228 offset:256\n"
"ds_read_b128 v[60:63], v233 offset:4096\n"
"ds_read_b128 v[64:67], v233 offset:36864\n"
"buffer_load_dwordx4 v226, s[36:39], s79 offen lds\n"
"s_add_u32 s79, s79, s78\n"
"v_mfma_f32_32x32x16_bf16 v[32:47], v[52:55], v[72:75], v[32:47]\n"
"s_waitcnt vmcnt(4) lgkmcnt(4)\n"
"s_add_u32 m0, s80, 6144\n"
"v_mfma_f32_32x32x16_bf16 v[16:31], v[56:59], v[76:79], v[16:31]\n"
"ds_read_b64_tr_b16 v[52:53], v228 offset:1024\n"
"ds_read_b64_tr_b16 v[54:55], v228 offset:1280\n"
"ds_read_b128 v[68:71], v234 offset:4096\n"
"ds_read_b128 v[72:75], v234 offset:36864\n"
"buffer_load_dwordx4 v226, s[36:39], s79 offen lds\n"
"s_add_u32 s79, s79, s78\n"
"v_mfma_f32_32x32x16_bf16 v[32:47], v[56:59], v[80:83], v[32:47]\n"
"s_waitcnt vmcnt(4) lgkmcnt(4)\n"
"s_add_u32 m0, s80, 0\n"
"v_mfma_f32_32x32x16_bf16 v[16:31], v[48:51], v[60:63], v[16:31]\n"
"ds_read_b64_tr_b16 v[56:57], v228 offset:2048\n"
"ds_read_b64_tr_b16 v[58:59], v228 offset:2304\n"
"ds_read_b128 v[76:79], v235 offset:4096\n"
"ds_read_b128 v[80:83], v235 offset:36864\n"
"buffer_load_dwordx4 v226, s[36:39], s79 offen lds\n"
"s_add_u32 s79, s79, s78\n"
"v_mfma_f32_32x32x16_bf16 v[32:47], v[48:51], v[64:67], v[32:47]\n"
"s_waitcnt vmcnt(4) lgkmcnt(4)\n"
"s_add_u32 m0, s80, 1024\n"
"v_mfma_f32_32x32x16_bf16 v[16:31], v[52:55], v[68:71], v[16:31]\n"
"ds_read_b64_tr_b16 v[48:49], v228 offset:3072\n"
"ds_read_b64_tr_b16 v[50:51], v228 offset:3328\n"
"s_cmp_eq_u32 s60, 1\n"
"s_mov_b32 s75, 8192\n"
"s_cselect_b32 s75, -24576, s75\n"
"v_add_u32 v232, s75, v232\n"
"v_add_u32 v233, s75, v233\n"
"v_add_u32 v234, s75, v234\n"
"v_add_u32 v235, s75, v235\n"
"ds_read_b128 v[60:63], v232 offset:0\n"
"ds_read_b128 v[64:67], v232 offset:32768\n"
"buffer_load_dwordx4 v226, s[36:39], s79 offen lds\n"
"s_add_u32 s79, s79, s78\n"
"v_mfma_f32_32x32x16_bf16 v[32:47], v[52:55], v[72:75], v[32:47]\n"
"s_waitcnt vmcnt(4) lgkmcnt(4)\n"
"s_add_u32 m0, s80, 2048\n"
"v_mfma_f32_32x32x16_bf16 v[16:31], v[56:59], v[76:79], v[16:31]\n"
"ds_read_b64_tr_b16 v[52:53], v228 offset:4096\n"
"ds_read_b64_tr_b16 v[54:55], v228 offset:4352\n"
"ds_read_b128 v[68:71], v233 offset:0\n"
"ds_read_b128 v[72:75], v233 offset:32768\n"
"buffer_load_dwordx4 v226, s[36:39], s79 offen lds\n"
"s_add_u32 s79, s79, s78\n"
"v_mfma_f32_32x32x16_bf16 v[32:47], v[56:59], v[80:83], v[32:47]\n"
"s_waitcnt vmcnt(4) lgkmcnt(4)\n"
"s_add_u32 m0, s80, 3072\n"
"v_mfma_f32_32x32x16_bf16 v[16:31], v[48:51], v[60:63], v[16:31]\n"
"ds_read_b64_tr_b16 v[56:57], v228 offset:5120\n"
"ds_read_b64_tr_b16 v[58:59], v228 offset:5376\n"
"ds_read_b128 v[76:79], v234 offset:0\n"
"ds_read_b128 v[80:83], v234 offset:32768\n"
"buffer_load_dwordx4 v226, s[36:39], s79 offen lds\n"
"s_add_u32 s79, s79, s78\n"
"v_mfma_f32_32x32x16_bf16 v[32:47], v[48:51], v[64:67], v[32:47]\n"
"s_waitcnt vmcnt(4) lgkmcnt(4)\n"
"v_mfma_f32_32x32x16_bf16 v[16:31], v[52:55], v[68:71], v[16:31]\n"
"ds_read_b64_tr_b16 v[48:49], v228 offset:6144\n"
"ds_read_b64_tr_b16 v[50:51], v228 offset:6400\n"
"ds_read_b128 v[60:63], v235 offset:0\n"
"ds_read_b128 v[64:67], v235 offset:32768\n"
"v_mfma_f32_32x32x16_bf16 v[32:47], v[52:55], v[72:75], v[32:47]\n"
"s_mov_b64 exec, 15\n"
"buffer_load_dwordx2 v[166:167], v231, s[40:43], s82 offen sc1\n"
"s_mov_b64 exec, -1\n"
"s_waitcnt vmcnt(4) lgkmcnt(4)\n"
"v_mfma_f32_32x32x16_bf16 v[16:31], v[56:59], v[76:79], v[16:31]\n"
"ds_read_b64_tr_b16 v[52:53], v228 offset:0\n"
"ds_read_b64_tr_b16 v[54:55], v228 offset:256\n"
"ds_read_b128 v[68:71], v232 offset:4096\n"
"ds_read_b128 v[72:75], v232 offset:36864\n"
"v_mfma_f32_32x32x16_bf16 v[32:47], v[56:59], v[80:83], v[32:47]\n"
"s_waitcnt vmcnt(3) lgkmcnt(4)\n"
"v_mfma_f32_32x32x16_bf16 v[16:31], v[48:51], v[60:63], v[16:31]\n"
"ds_read_b64_tr_b16 v[56:57], v228 offset:1024\n"
"ds_read_b64_tr_b16 v[58:59], v228 offset:1280\n"
"ds_read_b128 v[76:79], v233 offset:4096\n"
"ds_read_b128 v[80:83], v233 offset:36864\n"
"v_mfma_f32_32x32x16_bf16 v[32:47], v[48:51], v[64:67], v[32:47]\n"
"s_waitcnt vmcnt(2) lgkmcnt(4)\n"
"v_mfma_f32_32x32x16_bf16 v[16:31], v[52:55], v[68:71], v[16:31]\n"
"ds_read_b64_tr_b16 v[48:49], v228 offset:2048\n"
"ds_read_b64_tr_b16 v[50:51], v228 offset:2304\n"
"ds_read_b128 v[60:63], v234 offset:4096\n"
"ds_read_b128 v[64:67], v234 offset:36864\n"
"v_mfma_f32_32x32x16_bf16 v[32:47], v[52:55], v[72:75], v[32:47]\n"
"s_waitcnt vmcnt(1) lgkmcnt(4)\n"
"v_mfma_f32_32x32x16_bf16 v[16:31], v[56:59], v[76:79], v[16:31]\n"
"ds_read_b64_tr_b16 v[52:53], v228 offset:3072\n"
"ds_read_b64_tr_b16 v[54:55], v228 offset:3328\n"
"ds_read_b128 v[68:71], v235 offset:4096\n"
"ds_read_b128 v[72:75], v235 offset:36864\n"
"v_mfma_f32_32x32x16_bf16 v[32:47], v[56:59], v[80:83], v[32:47]\n"
"s_waitcnt lgkmcnt(4)\n"
"v_mfma_f32_32x32x16_bf16 v[16:31], v[48:51], v[60:63], v[16:31]\n"
"v_mfma_f32_32x32x16_bf16 v[32:47], v[48:51], v[64:67], v[32:47]\n"
"s_waitcnt lgkmcnt(0)\n"
"v_mfma_f32_32x32x16_bf16 v[16:31], v[52:55], v[68:71], v[16:31]\n"
"v_mfma_f32_32x32x16_bf16 v[32:47], v[52:55], v[72:75], v[32:47]\n"
"s_setprio 3\n"
"s_waitcnt vmcnt(0)\n"
"s_add_u32 s75, s84, 88064\n"
"s_nop 7\n"
"v_mul_f32 v16, v16, |v16|\n"
"v_mul_f32 v17, v17, |v17|\n"
"v_mul_f32 v18, v18, |v18|\n"
"v_mul_f32 v19, v19, |v19|\n"
"v_mul_f32 v20, v20, |v20|\n"
"v_mul_f32 v21, v21, |v21|\n"
"v_mul_f32 v22, v22, |v22|\n"
"v_mul_f32 v23, v23, |v23|\n"
"v_cvt_pk_bf16_f32 v84, v16, v17\n"
"v_cvt_pk_bf16_f32 v85, v18, v19\n"
"v_cvt_pk_bf16_f32 v86, v20, v21\n"
"v_cvt_pk_bf16_f32 v87, v22, v23\n"
"v_pk_max_i16 v84, v84, 0\n"
"v_pk_max_i16 v85, v85, 0\n"
"v_pk_max_i16 v86, v86, 0\n"
"v_pk_max_i16 v87, v87, 0\n"
"buffer_store_dwordx4 v[84:87], v2, s[40:43], s84 offen offset:0 sc1\n"
"v_mul_f32 v24, v24, |v24|\n"
"v_mul_f32 v25, v25, |v25|\n"
"v_mul_f32 v26, v26, |v26|\n"
"v_mul_f32 v27, v27, |v27|\n"
"v_mul_f32 v28, v28, |v28|\n"
"v_mul_f32 v29, v29, |v29|\n"
"v_mul_f32 v30, v30, |v30|\n"
"v_mul_f32 v31, v31, |v31|\n"
"v_cvt_pk_bf16_f32 v88, v24, v25\n"
"v_cvt_pk_bf16_f32 v89, v26, v27\n"
"v_cvt_pk_bf16_f32 v90, v28, v29\n"
"v_cvt_pk_bf16_f32 v91, v30, v31\n"
"v_pk_max_i16 v88, v88, 0\n"
"v_pk_max_i16 v89, v89, 0\n"
"v_pk_max_i16 v90, v90, 0\n"
"v_pk_max_i16 v91, v91, 0\n"
"buffer_store_dwordx4 v[88:91], v2, s[40:43], s84 offen offset:1024 sc1\n"
"v_mul_f32 v32, v32, |v32|\n"
"v_mul_f32 v33, v33, |v33|\n"
"v_mul_f32 v34, v34, |v34|\n"
"v_mul_f32 v35, v35, |v35|\n"
"v_mul_f32 v36, v36, |v36|\n"
"v_mul_f32 v37, v37, |v37|\n"
"v_mul_f32 v38, v38, |v38|\n"
"v_mul_f32 v39, v39, |v39|\n"
"v_cvt_pk_bf16_f32 v92, v32, v33\n"
"v_cvt_pk_bf16_f32 v93, v34, v35\n"
"v_cvt_pk_bf16_f32 v94, v36, v37\n"
"v_cvt_pk_bf16_f32 v95, v38, v39\n"
"v_pk_max_i16 v92, v92, 0\n"
"v_pk_max_i16 v93, v93, 0\n"
"v_pk_max_i16 v94, v94, 0\n"
"v_pk_max_i16 v95, v95, 0\n"
"buffer_store_dwordx4 v[92:95], v2, s[40:43], s75 offen offset:0 sc1\n"
"v_mul_f32 v40, v40, |v40|\n"
"v_mul_f32 v41, v41, |v41|\n"
"v_mul_f32 v42, v42, |v42|\n"
"v_mul_f32 v43, v43, |v43|\n"
"v_mul_f32 v44, v44, |v44|\n"
"v_mul_f32 v45, v45, |v45|\n"
"v_mul_f32 v46, v46, |v46|\n"
"v_mul_f32 v47, v47, |v47|\n"
"v_cvt_pk_bf16_f32 v96, v40, v41\n"
"v_cvt_pk_bf16_f32 v97, v42, v43\n"
"v_cvt_pk_bf16_f32 v98, v44, v45\n"
"v_cvt_pk_bf16_f32 v99, v46, v47\n"
"v_pk_max_i16 v96, v96, 0\n"
"v_pk_max_i16 v97, v97, 0\n"
"v_pk_max_i16 v98, v98, 0\n"
"v_pk_max_i16 v99, v99, 0\n"
"buffer_store_dwordx4 v[96:99], v2, s[40:43], s75 offen offset:1024 sc1\n"
"s_setprio 1\n"
"s_mov_b64 exec, 15\n"
"v_cmp_ne_u64 vcc, s[72:73], v[166:167]\n"
"s_mov_b64 exec, -1\n"
"s_cbranch_vccz Lbjoin_72_%=\n"
"s_branch Lbspin_105_%=\n"
"Lbnotile_71_%=:\n"
"s_add_u32 m0, s83, 69632\n"
"s_add_u32 s75, s83, 4096\n"
"buffer_load_dwordx4 v2, s[40:43], s75 offen offset:0 sc1 lds\n"
"buffer_load_dwordx4 v2, s[40:43], s75 offen offset:1024 sc1 lds\n"
"buffer_load_dwordx4 v2, s[40:43], s75 offen offset:2048 sc1 lds\n"
"buffer_load_dwordx4 v2, s[40:43], s75 offen offset:3072 sc1 lds\n"
"s_add_u32 m0, s83, 73728\n"
"s_add_u32 s75, s83, 8192\n"
"buffer_load_dwordx4 v2, s[40:43], s75 offen offset:0 sc1 lds\n"
"buffer_load_dwordx4 v2, s[40:43], s75 offen offset:1024 sc1 lds\n"
"buffer_load_dwordx4 v2, s[40:43], s75 offen offset:2048 sc1 lds\n"
"buffer_load_dwordx4 v2, s[40:43], s75 offen offset:3072 sc1 lds\n"
"s_waitcnt vmcnt(0) lgkmcnt(0)\n"
"s_barrier\n"
"s_add_u32 m0, s83, 77824\n"
"s_add_u32 s75, s83, 12288\n"
"buffer_load_dwordx4 v2, s[40:43], s75 offen offset:0 sc1 lds\n"
"buffer_load_dwordx4 v2, s[40:43], s75 offen offset:1024 sc1 lds\n"
"buffer_load_dwordx4 v2, s[40:43], s75 offen offset:2048 sc1 lds\n"
"buffer_load_dwordx4 v2, s[40:43], s75 offen offset:3072 sc1 lds\n"
"Lbspin_105_%=:\n"
"s_mov_b32 s74, 524288\n"
"Lspin_106_%=:\n"
"s_mov_b64 exec, 15\n"
"buffer_load_dwordx2 v[166:167], v231, s[40:43], s82 offen sc1\n"
"s_mov_b64 exec, -1\n"
"Ltst_108_%=:\n"
"s_waitcnt vmcnt(0)\n"
"s_mov_b64 exec, 15\n"
"v_cmp_ne_u64 vcc, s[72:73], v[166:167]\n"
"s_mov_b64 exec, -1\n"
"s_cbranch_vccz Lgot_107_%=\n"
"s_sleep 2\n"
"s_sub_u32 s74, s74, 1\n"
"s_cmp_lg_u32 s74, 0\n"
"s_cbranch_scc1 Lspin_106_%=\n"
"Lgot_107_%=:\n"
"Lbjoin_72_%=:\n"
"s_mov_b32 s60, s24\n"
"s_and_b32 s61, s25, 0xffff\n"
"s_mov_b32 s62, 1024\n"
"s_mov_b32 s63, 0x20000\n"
"s_setprio 1\n"
"s_waitcnt vmcnt(4) lgkmcnt(0)\n"
"s_barrier\n"
"s_add_u32 s74, s83, 65536\n"
"s_add_u32 m0, s83, 0\n"
"s_add_u32 s75, s74, 0\n"
"buffer_load_dwordx4 v2, s[40:43], s75 offen offset:0 sc1 lds\n"
"buffer_load_dwordx4 v2, s[40:43], s75 offen offset:1024 sc1 lds\n"
"buffer_load_dwordx4 v2, s[40:43], s75 offen offset:2048 sc1 lds\n"
"buffer_load_dwordx4 v2, s[40:43], s75 offen offset:3072 sc1 lds\n"
"s_add_u32 m0, s83, 4096\n"
"s_add_u32 s75, s74, 4096\n"
"buffer_load_dwordx4 v2, s[40:43], s75 offen offset:0 sc1 lds\n"
"buffer_load_dwordx4 v2, s[40:43], s75 offen offset:1024 sc1 lds\n"
"buffer_load_dwordx4 v2, s[40:43], s75 offen offset:2048 sc1 lds\n"
"buffer_load_dwordx4 v2, s[40:43], s75 offen offset:3072 sc1 lds\n"
"s_add_u32 m0, s83, 8192\n"
"s_add_u32 s75, s74, 8192\n"
"buffer_load_dwordx4 v2, s[40:43], s75 offen offset:0 sc1 lds\n"
"buffer_load_dwordx4 v2, s[40:43], s75 offen offset:1024 sc1 lds\n"
"buffer_load_dwordx4 v2, s[40:43], s75 offen offset:2048 sc1 lds\n"
"buffer_load_dwordx4 v2, s[40:43], s75 offen offset:3072 sc1 lds\n"
"s_add_u32 m0, s83, 12288\n"
"s_add_u32 s75, s74, 12288\n"
"buffer_load_dwordx4 v2, s[40:43], s75 offen offset:0 sc1 lds\n"
"buffer_load_dwordx4 v2, s[40:43], s75 offen offset:1024 sc1 lds\n"
"buffer_load_dwordx4 v2, s[40:43], s75 offen offset:2048 sc1 lds\n"
"buffer_load_dwordx4 v2, s[40:43], s75 offen offset:3072 sc1 lds\n"
"s_waitcnt vmcnt(16)\n"
"s_mov_b64 exec, 1\n"
"buffer_store_dwordx2 v[204:205], v206, s[40:43], s85 offen sc1\n"
"s_mov_b64 exec, -1\n"
"s_and_b32 s74, s70, 3\n"
"s_lshl_b32 s74, s74, 2\n"
"s_add_u32 s74, s74, s71\n"
"s_mul_i32 s75, s74, 88064\n"
"s_lshl_b32 s76, s69, 7\n"
"s_add_u32 s75, s75, s76\n"
"s_lshl_b32 s74, s74, 15\n"
"s_add_u32 s74, s74, s76\n"
"v_lshrrev_b32 v4, 1, v1\n"
"v_lshlrev_b32 v4, 10, v4\n"
"v_and_b32 v5, 1, v1\n"
"v_lshl_add_u32 v4, v5, 6, v4\n"
"buffer_load_dword v254, v4, s[44:47], s75 offen\n"
"s_add_u32 s75, s75, 32768\n"
"buffer_load_dword v254, v4, s[44:47], s75 offen\n"
"s_add_u32 s75, s75, 32768\n"
"buffer_load_dword v254, v4, s[44:47], s75 offen\n"
"s_add_u32 s75, s75, 32768\n"
"buffer_load_dword v254, v4, s[48:51], s74 offen\n"
"ds_read2st64_b32 v[4:5], v241 offset0:0 offset1:1\n"
"s_waitcnt lgkmcnt(0)\n"
"v_mov_b32 v48, v4\n"
"v_mov_b32 v64, v5\n"
"v_mov_b32 v49, v48\n"
"v_pk_mov_b32 v[50:51], v[48:49], v[48:49]\n"
"v_pk_mov_b32 v[52:53], v[48:49], v[48:49]\n"
"v_pk_mov_b32 v[54:55], v[48:49], v[48:49]\n"
"v_pk_mov_b32 v[56:57], v[48:49], v[48:49]\n"
"v_pk_mov_b32 v[58:59], v[48:49], v[48:49]\n"
"v_pk_mov_b32 v[60:61], v[48:49], v[48:49]\n"
"v_pk_mov_b32 v[62:63], v[48:49], v[48:49]\n"
"v_mov_b32 v65, v64\n"
"v_pk_mov_b32 v[66:67], v[64:65], v[64:65]\n"
"v_pk_mov_b32 v[68:69], v[64:65], v[64:65]\n"
"v_pk_mov_b32 v[70:71], v[64:65], v[64:65]\n"
"v_pk_mov_b32 v[72:73], v[64:65], v[64:65]\n"
"v_pk_mov_b32 v[74:75], v[64:65], v[64:65]\n"
"v_pk_mov_b32 v[76:77], v[64:65], v[64:65]\n"
"v_pk_mov_b32 v[78:79], v[64:65], v[64:65]\n"
"s_waitcnt vmcnt(5)\n"
"s_barrier\n"
"ds_read_b128 v[140:143], v236 offset:0\n"
"ds_read_b128 v[144:147], v236 offset:16384\n"
"s_mov_b64 exec, s[90:91]\n"
"ds_read_b128 v[148:151], v238 offset:0\n"
"ds_read_b128 v[152:155], v238 offset:512\n"
"s_mov_b64 exec, -1\n"
"s_waitcnt lgkmcnt(2)\n"
"v_mfma_f32_32x32x16_bf16 v[16:31], v[140:143], v[132:135], v[48:63]\n"
"Lattn_31_%=:\n"
"s_nop 11\n"
"v_exp_f32 v16, v16\n"
"v_exp_f32 v17, v17\n"
"s_waitcnt lgkmcnt(0)\n"
"v_exp_f32 v18, v18\n"
"v_exp_f32 v19, v19\n"
"v_exp_f32 v20, v20\n"
"v_mfma_f32_32x32x16_bf16 v[32:47], v[140:143], v[136:139], v[64:79]\n"
"v_exp_f32 v21, v21\n"
"v_exp_f32 v22, v22\n"
"v_exp_f32 v23, v23\n"
"v_exp_f32 v24, v24\n"
"v_exp_f32 v25, v25\n"
"v_exp_f32 v26, v26\n"
"v_smfmac_f32_32x32x32_bf16 v[100:115], v[148:151], v[16:23], v164\n"
"s_mov_b64 exec, s[90:91]\n"
"ds_read_b128 v[156:159], v238 offset:16384\n"
"ds_read_b128 v[160:163], v238 offset:16896\n"
"s_mov_b64 exec, -1\n"
"v_exp_f32 v27, v27\n"
"v_exp_f32 v28, v28\n"
"v_exp_f32 v29, v29\n"
"v_exp_f32 v30, v30\n"
"v_exp_f32 v31, v31\n"
"v_exp_f32 v32, v32\n"
"v_exp_f32 v33, v33\n"
"v_smfmac_f32_32x32x32_bf16 v[100:115], v[152:155], v[24:31], v164\n"
"v_exp_f32 v34, v34\n"
"v_exp_f32 v35, v35\n"
"v_exp_f32 v36, v36\n"
"v_mfma_f32_32x32x16_bf16 v[16:31], v[144:147], v[132:135], v[48:63]\n"
"v_exp_f32 v37, v37\n"
"v_exp_f32 v38, v38\n"
"v_exp_f32 v39, v39\n"
"v_exp_f32 v40, v40\n"
"v_exp_f32 v41, v41\n"
"v_exp_f32 v42, v42\n"
"v_smfmac_f32_32x32x32_bf16 v[116:131], v[148:151], v[32:39], v164\n"
"ds_read_b128 v[140:143], v237 offset:0\n"
"v_exp_f32 v43, v43\n"
"v_exp_f32 v44, v44\n"
"v_exp_f32 v45, v45\n"
"v_exp_f32 v46, v46\n"
"v_exp_f32 v47, v47\n"
"v_exp_f32 v16, v16\n"
"v_exp_f32 v17, v17\n"
"v_smfmac_f32_32x32x32_bf16 v[116:131], v[152:155], v[40:47], v164\n"
"s_waitcnt lgkmcnt(0)\n"
"v_exp_f32 v18, v18\n"
"v_exp_f32 v19, v19\n"
"v_exp_f32 v20, v20\n"
"v_mfma_f32_32x32x16_bf16 v[32:47], v[144:147], v[136:139], v[64:79]\n"
"v_exp_f32 v21, v21\n"
"v_exp_f32 v22, v22\n"
"v_exp_f32 v23, v23\n"
"v_exp_f32 v24, v24\n"
"v_exp_f32 v25, v25\n"
"v_exp_f32 v26, v26\n"
"v_smfmac_f32_32x32x32_bf16 v[100:115], v[156:159], v[16:23], v164\n"
"s_mov_b64 exec, s[90:91]\n"
"ds_read_b128 v[148:151], v239 offset:0\n"
"ds_read_b128 v[152:155], v239 offset:512\n"
"s_mov_b64 exec, -1\n"
"v_exp_f32 v27, v27\n"
"v_exp_f32 v28, v28\n"
"v_exp_f32 v29, v29\n"
"v_exp_f32 v30, v30\n"
"v_exp_f32 v31, v31\n"
"v_exp_f32 v32, v32\n"
"v_exp_f32 v33, v33\n"
"v_smfmac_f32_32x32x32_bf16 v[100:115], v[160:163], v[24:31], v164\n"
"v_exp_f32 v34, v34\n"
"v_exp_f32 v35, v35\n"
"v_exp_f32 v36, v36\n"
"v_mfma_f32_32x32x16_bf16 v[16:31], v[140:143], v[132:135], v[48:63]\n"
"v_exp_f32 v37, v37\n"
"v_exp_f32 v38, v38\n"
"v_exp_f32 v39, v39\n"
"v_exp_f32 v40, v40\n"
"v_exp_f32 v41, v41\n"
"v_exp_f32 v42, v42\n"
"v_smfmac_f32_32x32x32_bf16 v[116:131], v[156:159], v[32:39], v164\n"
"ds_read_b128 v[144:147], v237 offset:16384\n"
"v_exp_f32 v43, v43\n"
"v_exp_f32 v44, v44\n"
"v_exp_f32 v45, v45\n"
"v_exp_f32 v46, v46\n"
"v_exp_f32 v47, v47\n"
"v_exp_f32 v16, v16\n"
"v_exp_f32 v17, v17\n"
"v_smfmac_f32_32x32x32_bf16 v[116:131], v[160:163], v[40:47], v164\n"
"s_waitcnt lgkmcnt(0)\n"
"v_exp_f32 v18, v18\n"
"v_exp_f32 v19, v19\n"
"v_exp_f32 v20, v20\n"
"v_mfma_f32_32x32x16_bf16 v[32:47], v[140:143], v[136:139], v[64:79]\n"
"v_exp_f32 v21, v21\n"
"v_exp_f32 v22, v22\n"
"v_exp_f32 v23, v23\n"
"v_exp_f32 v24, v24\n"
"v_exp_f32 v25, v25\n"
"v_exp_f32 v26, v26\n"
"v_smfmac_f32_32x32x32_bf16 v[100:115], v[148:151], v[16:23], v164\n"
"s_mov_b64 exec, s[90:91]\n"
"ds_read_b128 v[156:159], v239 offset:16384\n"
"ds_read_b128 v[160:163], v239 offset:16896\n"
"s_mov_b64 exec, -1\n"
"v_exp_f32 v27, v27\n"
"v_exp_f32 v28, v28\n"
"v_exp_f32 v29, v29\n"
"v_exp_f32 v30, v30\n"
"v_exp_f32 v31, v31\n"
"v_exp_f32 v32, v32\n"
"v_exp_f32 v33, v33\n"
"v_smfmac_f32_32x32x32_bf16 v[100:115], v[152:155], v[24:31], v164\n"
"v_exp_f32 v34, v34\n"
"v_exp_f32 v35, v35\n"
"v_exp_f32 v36, v36\n"
"v_mfma_f32_32x32x16_bf16 v[16:31], v[144:147], v[132:135], v[48:63]\n"
"v_exp_f32 v37, v37\n"
"v_exp_f32 v38, v38\n"
"v_exp_f32 v39, v39\n"
"v_exp_f32 v40, v40\n"
"v_exp_f32 v41, v41\n"
"v_exp_f32 v42, v42\n"
"v_smfmac_f32_32x32x32_bf16 v[116:131], v[148:151], v[32:39], v164\n"
"ds_read_b128 v[140:143], v236 offset:2048\n"
"v_exp_f32 v43, v43\n"
"v_exp_f32 v44, v44\n"
"v_exp_f32 v45, v45\n"
"v_exp_f32 v46, v46\n"
"v_exp_f32 v47, v47\n"
"v_exp_f32 v16, v16\n"
"v_exp_f32 v17, v17\n"
"v_smfmac_f32_32x32x32_bf16 v[116:131], v[152:155], v[40:47], v164\n"
"s_waitcnt lgkmcnt(0)\n"
"v_exp_f32 v18, v18\n"
"v_exp_f32 v19, v19\n"
"v_exp_f32 v20, v20\n"
"v_mfma_f32_32x32x16_bf16 v[32:47], v[144:147], v[136:139], v[64:79]\n"
"v_exp_f32 v21, v21\n"
"v_exp_f32 v22, v22\n"
"v_exp_f32 v23, v23\n"
"v_exp_f32 v24, v24\n"
"v_exp_f32 v25, v25\n"
"v_exp_f32 v26, v26\n"
"v_smfmac_f32_32x32x32_bf16 v[100:115], v[156:159], v[16:23], v164\n"
"s_mov_b64 exec, s[90:91]\n"
"ds_read_b128 v[148:151], v238 offset:2048\n"
"ds_read_b128 v[152:155], v238 offset:2560\n"
"s_mov_b64 exec, -1\n"
"v_exp_f32 v27, v27\n"
"v_exp_f32 v28, v28\n"
"v_exp_f32 v29, v29\n"
"v_exp_f32 v30, v30\n"
"v_exp_f32 v31, v31\n"
"v_exp_f32 v32, v32\n"
"v_exp_f32 v33, v33\n"
"v_smfmac_f32_32x32x32_bf16 v[100:115], v[160:163], v[24:31], v164\n"
"v_exp_f32 v34, v34\n"
"v_exp_f32 v35, v35\n"
"v_exp_f32 v36, v36\n"
"v_mfma_f32_32x32x16_bf16 v[16:31], v[140:143], v[132:135], v[48:63]\n"
"v_exp_f32 v37, v37\n"
"v_exp_f32 v38, v38\n"
"v_exp_f32 v39, v39\n"
"v_exp_f32 v40, v40\n"
"v_exp_f32 v41, v41\n"
"v_exp_f32 v42, v42\n"
"v_smfmac_f32_32x32x32_bf16 v[116:131], v[156:159], v[32:39], v164\n"
"ds_read_b128 v[144:147], v236 offset:18432\n"
"v_exp_f32 v43, v43\n"
"v_exp_f32 v44, v44\n"
"v_exp_f32 v45, v45\n"
"v_exp_f32 v46, v46\n"
"v_exp_f32 v47, v47\n"
"v_exp_f32 v16, v16\n"
"v_exp_f32 v17, v17\n"
"v_smfmac_f32_32x32x32_bf16 v[116:131], v[160:163], v[40:47], v164\n"
"s_waitcnt lgkmcnt(0)\n"
"v_exp_f32 v18, v18\n"
"v_exp_f32 v19, v19\n"
"v_exp_f32 v20, v20\n"
"v_mfma_f32_32x32x16_bf16 v[32:47], v[140:143], v[136:139], v[64:79]\n"
"v_exp_f32 v21, v21\n"
"v_exp_f32 v22, v22\n"
"v_exp_f32 v23, v23\n"
"v_exp_f32 v24, v24\n"
"v_exp_f32 v25, v25\n"
"v_exp_f32 v26, v26\n"
"v_smfmac_f32_32x32x32_bf16 v[100:115], v[148:151], v[16:23], v164\n"
"s_mov_b64 exec, s[90:91]\n"
"ds_read_b128 v[156:159], v238 offset:18432\n"
"ds_read_b128 v[160:163], v238 offset:18944\n"
"s_mov_b64 exec, -1\n"
"v_exp_f32 v27, v27\n"
"v_exp_f32 v28, v28\n"
"v_exp_f32 v29, v29\n"
"v_exp_f32 v30, v30\n"
"v_exp_f32 v31, v31\n"
"v_exp_f32 v32, v32\n"
"v_exp_f32 v33, v33\n"
"v_smfmac_f32_32x32x32_bf16 v[100:115], v[152:155], v[24:31], v164\n"
"v_exp_f32 v34, v34\n"
"v_exp_f32 v35, v35\n"
"v_exp_f32 v36, v36\n"
"v_mfma_f32_32x32x16_bf16 v[16:31], v[144:147], v[132:135], v[48:63]\n"
"v_exp_f32 v37, v37\n"
"v_exp_f32 v38, v38\n"
"v_exp_f32 v39, v39\n"
"v_exp_f32 v40, v40\n"
"v_exp_f32 v41, v41\n"
"v_exp_f32 v42, v42\n"
"v_smfmac_f32_32x32x32_bf16 v[116:131], v[148:151], v[32:39], v164\n"
"ds_read_b128 v[140:143], v237 offset:2048\n"
"v_exp_f32 v43, v43\n"
"v_exp_f32 v44, v44\n"
"v_exp_f32 v45, v45\n"
"v_exp_f32 v46, v46\n"
"v_exp_f32 v47, v47\n"
"v_exp_f32 v16, v16\n"
"v_exp_f32 v17, v17\n"
"v_smfmac_f32_32x32x32_bf16 v[116:131], v[152:155], v[40:47], v164\n"
"s_waitcnt lgkmcnt(0)\n"
"v_exp_f32 v18, v18\n"
"v_exp_f32 v19, v19\n"
"v_exp_f32 v20, v20\n"
"v_mfma_f32_32x32x16_bf16 v[32:47], v[144:147], v[136:139], v[64:79]\n"
"v_exp_f32 v21, v21\n"
"v_exp_f32 v22, v22\n"
"v_exp_f32 v23, v23\n"
"v_exp_f32 v24, v24\n"
"v_exp_f32 v25, v25\n"
"v_exp_f32 v26, v26\n"
"v_smfmac_f32_32x32x32_bf16 v[100:115], v[156:159], v[16:23], v164\n"
"s_mov_b64 exec, s[90:91]\n"
"ds_read_b128 v[148:151], v239 offset:2048\n"
"ds_read_b128 v[152:155], v239 offset:2560\n"
"s_mov_b64 exec, -1\n"
"v_exp_f32 v27, v27\n"
"v_exp_f32 v28, v28\n"
"v_exp_f32 v29, v29\n"
"v_exp_f32 v30, v30\n"
"v_exp_f32 v31, v31\n"
"v_exp_f32 v32, v32\n"
"v_exp_f32 v33, v33\n"
"v_smfmac_f32_32x32x32_bf16 v[100:115], v[160:163], v[24:31], v164\n"
"s_cmp_ge_u32 s68, 4\n"
"s_cbranch_scc1 Lcls_109_%=\n"
"s_waitcnt vmcnt(0)\n"
"s_barrier\n"
"s_mov_b64 exec, 1\n"
"buffer_store_dwordx2 v[204:205], v206, s[40:43], s85 offen sc1\n"
"s_mov_b64 exec, -1\n"
"Lcls_109_%=:\n"
"v_exp_f32 v34, v34\n"
"v_exp_f32 v35, v35\n"
"v_exp_f32 v36, v36\n"
"v_mfma_f32_32x32x16_bf16 v[16:31], v[140:143], v[132:135], v[48:63]\n"
"v_exp_f32 v37, v37\n"
"v_exp_f32 v38, v38\n"
"v_exp_f32 v39, v39\n"
"v_exp_f32 v40, v40\n"
"v_exp_f32 v41, v41\n"
"v_exp_f32 v42, v42\n"
"v_smfmac_f32_32x32x32_bf16 v[116:131], v[156:159], v[32:39], v164\n"
"ds_read_b128 v[144:147], v237 offset:18432\n"
"v_exp_f32 v43, v43\n"
"v_exp_f32 v44, v44\n"
"v_exp_f32 v45, v45\n"
"v_exp_f32 v46, v46\n"
"v_exp_f32 v47, v47\n"
"v_exp_f32 v16, v16\n"
"v_exp_f32 v17, v17\n"
"v_smfmac_f32_32x32x32_bf16 v[116:131], v[160:163], v[40:47], v164\n"
"s_waitcnt lgkmcnt(0)\n"
"v_exp_f32 v18, v18\n"
"v_exp_f32 v19, v19\n"
"v_exp_f32 v20, v20\n"
"v_mfma_f32_32x32x16_bf16 v[32:47], v[140:143], v[136:139], v[64:79]\n"
"v_exp_f32 v21, v21\n"
"v_exp_f32 v22, v22\n"
"v_exp_f32 v23, v23\n"
"v_exp_f32 v24, v24\n"
"v_exp_f32 v25, v25\n"
"v_exp_f32 v26, v26\n"
"v_smfmac_f32_32x32x32_bf16 v[100:115], v[148:151], v[16:23], v164\n"
"s_mov_b64 exec, s[90:91]\n"
"ds_read_b128 v[156:159], v239 offset:18432\n"
"ds_read_b128 v[160:163], v239 offset:18944\n"
"s_mov_b64 exec, -1\n"
"v_exp_f32 v27, v27\n"
"v_exp_f32 v28, v28\n"
"v_exp_f32 v29, v29\n"
"v_exp_f32 v30, v30\n"
"v_exp_f32 v31, v31\n"
"v_exp_f32 v32, v32\n"
"v_exp_f32 v33, v33\n"
"v_smfmac_f32_32x32x32_bf16 v[100:115], v[152:155], v[24:31], v164\n"
"v_exp_f32 v34, v34\n"
"v_exp_f32 v35, v35\n"
"v_exp_f32 v36, v36\n"
"v_mfma_f32_32x32x16_bf16 v[16:31], v[144:147], v[132:135], v[48:63]\n"
"v_exp_f32 v37, v37\n"
"v_exp_f32 v38, v38\n"
"v_exp_f32 v39, v39\n"
"v_exp_f32 v40, v40\n"
"v_exp_f32 v41, v41\n"
"v_exp_f32 v42, v42\n"
"v_smfmac_f32_32x32x32_bf16 v[116:131], v[148:151], v[32:39], v164\n"
"ds_read_b128 v[140:143], v236 offset:4096\n"
"v_exp_f32 v43, v43\n"
"v_exp_f32 v44, v44\n"
"v_exp_f32 v45, v45\n"
"v_exp_f32 v46, v46\n"
"v_exp_f32 v47, v47\n"
"v_exp_f32 v16, v16\n"
"v_exp_f32 v17, v17\n"
"v_smfmac_f32_32x32x32_bf16 v[116:131], v[152:155], v[40:47], v164\n"
"s_waitcnt lgkmcnt(0)\n"
"v_exp_f32 v18, v18\n"
"v_exp_f32 v19, v19\n"
"v_exp_f32 v20, v20\n"
"v_mfma_f32_32x32x16_bf16 v[32:47], v[144:147], v[136:139], v[64:79]\n"
"v_exp_f32 v21, v21\n"
"v_exp_f32 v22, v22\n"
"v_exp_f32 v23, v23\n"
"v_exp_f32 v24, v24\n"
"v_exp_f32 v25, v25\n"
"v_exp_f32 v26, v26\n"
"v_smfmac_f32_32x32x32_bf16 v[100:115], v[156:159], v[16:23], v164\n"
"s_mov_b64 exec, s[90:91]\n"
"ds_read_b128 v[148:151], v238 offset:4096\n"
"ds_read_b128 v[152:155], v238 offset:4608\n"
"s_mov_b64 exec, -1\n"
"v_exp_f32 v27, v27\n"
"v_exp_f32 v28, v28\n"
"v_exp_f32 v29, v29\n"
"v_exp_f32 v30, v30\n"
"v_exp_f32 v31, v31\n"
"v_exp_f32 v32, v32\n"
"v_exp_f32 v33, v33\n"
"v_smfmac_f32_32x32x32_bf16 v[100:115], v[160:163], v[24:31], v164\n"
"v_exp_f32 v34, v34\n"
"v_exp_f32 v35, v35\n"
"v_exp_f32 v36, v36\n"
"v_mfma_f32_32x32x16_bf16 v[16:31], v[140:143], v[132:135], v[48:63]\n"
"v_exp_f32 v37, v37\n"
"v_exp_f32 v38, v38\n"
"v_exp_f32 v39, v39\n"
"v_exp_f32 v40, v40\n"
"v_exp_f32 v41, v41\n"
"v_exp_f32 v42, v42\n"
"v_smfmac_f32_32x32x32_bf16 v[116:131], v[156:159], v[32:39], v164\n"
"ds_read_b128 v[144:147], v236 offset:20480\n"
"v_exp_f32 v43, v43\n"
"v_exp_f32 v44, v44\n"
"v_exp_f32 v45, v45\n"
"v_exp_f32 v46, v46\n"
"v_exp_f32 v47, v47\n"
"v_exp_f32 v16, v16\n"
"v_exp_f32 v17, v17\n"
"v_smfmac_f32_32x32x32_bf16 v[116:131], v[160:163], v[40:47], v164\n"
"s_waitcnt lgkmcnt(0)\n"
"v_exp_f32 v18, v18\n"
"v_exp_f32 v19, v19\n"
"v_exp_f32 v20, v20\n"
"v_mfma_f32_32x32x16_bf16 v[32:47], v[140:143], v[136:139], v[64:79]\n"
"v_exp_f32 v21, v21\n"
"v_exp_f32 v22, v22\n"
"v_exp_f32 v23, v23\n"
"v_exp_f32 v24, v24\n"
"v_exp_f32 v25, v25\n"
"v_exp_f32 v26, v26\n"
"v_smfmac_f32_32x32x32_bf16 v[100:115], v[148:151], v[16:23], v164\n"
"s_mov_b64 exec, s[90:91]\n"
"ds_read_b128 v[156:159], v238 offset:20480\n"
"ds_read_b128 v[160:163], v238 offset:20992\n"
"s_mov_b64 exec, -1\n"
"v_exp_f32 v27, v27\n"
"v_exp_f32 v28, v28\n"
"v_exp_f32 v29, v29\n"
"v_exp_f32 v30, v30\n"
"v_exp_f32 v31, v31\n"
"v_exp_f32 v32, v32\n"
"v_exp_f32 v33, v33\n"
"v_smfmac_f32_32x32x32_bf16 v[100:115], v[152:155], v[24:31], v164\n"
"s_cmp_ge_u32 s68, 4\n"
"s_cbranch_scc0 Lcls_110_%=\n"
"s_barrier\n"
"Lcls_110_%=:\n"
"v_exp_f32 v34, v34\n"
"v_exp_f32 v35, v35\n"
"v_exp_f32 v36, v36\n"
"v_mfma_f32_32x32x16_bf16 v[16:31], v[144:147], v[132:135], v[48:63]\n"
"v_exp_f32 v37, v37\n"
"v_exp_f32 v38, v38\n"
"v_exp_f32 v39, v39\n"
"v_exp_f32 v40, v40\n"
"v_exp_f32 v41, v41\n"
"v_exp_f32 v42, v42\n"
"v_smfmac_f32_32x32x32_bf16 v[116:131], v[148:151], v[32:39], v164\n"
"ds_read_b128 v[140:143], v237 offset:4096\n"
"v_exp_f32 v43, v43\n"
"v_exp_f32 v44, v44\n"
"v_exp_f32 v45, v45\n"
"v_exp_f32 v46, v46\n"
"v_exp_f32 v47, v47\n"
"v_exp_f32 v16, v16\n"
"v_exp_f32 v17, v17\n"
"v_smfmac_f32_32x32x32_bf16 v[116:131], v[152:155], v[40:47], v164\n"
"s_waitcnt lgkmcnt(0)\n"
"v_exp_f32 v18, v18\n"
"v_exp_f32 v19, v19\n"
"v_exp_f32 v20, v20\n"
"v_mfma_f32_32x32x16_bf16 v[32:47], v[144:147], v[136:139], v[64:79]\n"
"v_exp_f32 v21, v21\n"
"v_exp_f32 v22, v22\n"
"v_exp_f32 v23, v23\n"
"v_exp_f32 v24, v24\n"
"v_exp_f32 v25, v25\n"
"v_exp_f32 v26, v26\n"
"v_smfmac_f32_32x32x32_bf16 v[100:115], v[156:159], v[16:23], v164\n"
"s_mov_b64 exec, s[90:91]\n"
"ds_read_b128 v[148:151], v239 offset:4096\n"
"ds_read_b128 v[152:155], v239 offset:4608\n"
"s_mov_b64 exec, -1\n"
"v_exp_f32 v27, v27\n"
"v_exp_f32 v28, v28\n"
"v_exp_f32 v29, v29\n"
"v_exp_f32 v30, v30\n"
"v_exp_f32 v31, v31\n"
"v_exp_f32 v32, v32\n"
"v_exp_f32 v33, v33\n"
"v_smfmac_f32_32x32x32_bf16 v[100:115], v[160:163], v[24:31], v164\n"
"v_exp_f32 v34, v34\n"
"v_exp_f32 v35, v35\n"
"v_exp_f32 v36, v36\n"
"v_mfma_f32_32x32x16_bf16 v[16:31], v[140:143], v[132:135], v[48:63]\n"
"v_exp_f32 v37, v37\n"
"v_exp_f32 v38, v38\n"
"v_exp_f32 v39, v39\n"
"v_exp_f32 v40, v40\n"
"v_exp_f32 v41, v41\n"
"v_exp_f32 v42, v42\n"
"v_smfmac_f32_32x32x32_bf16 v[116:131], v[156:159], v[32:39], v164\n"
"ds_read_b128 v[144:147], v237 offset:20480\n"
"v_exp_f32 v43, v43\n"
"v_exp_f32 v44, v44\n"
"v_exp_f32 v45, v45\n"
"v_exp_f32 v46, v46\n"
"v_exp_f32 v47, v47\n"
"v_exp_f32 v16, v16\n"
"v_exp_f32 v17, v17\n"
"v_smfmac_f32_32x32x32_bf16 v[116:131], v[160:163], v[40:47], v164\n"
"s_waitcnt lgkmcnt(0)\n"
"v_exp_f32 v18, v18\n"
"v_exp_f32 v19, v19\n"
"v_exp_f32 v20, v20\n"
"v_mfma_f32_32x32x16_bf16 v[32:47], v[140:143], v[136:139], v[64:79]\n"
"v_exp_f32 v21, v21\n"
"v_exp_f32 v22, v22\n"
"v_exp_f32 v23, v23\n"
"v_exp_f32 v24, v24\n"
"v_exp_f32 v25, v25\n"
"v_exp_f32 v26, v26\n"
"v_smfmac_f32_32x32x32_bf16 v[100:115], v[148:151], v[16:23], v164\n"
"s_mov_b64 exec, s[90:91]\n"
"ds_read_b128 v[156:159], v239 offset:20480\n"
"ds_read_b128 v[160:163], v239 offset:20992\n"
"s_mov_b64 exec, -1\n"
"v_exp_f32 v27, v27\n"
"v_exp_f32 v28, v28\n"
"v_exp_f32 v29, v29\n"
"v_exp_f32 v30, v30\n"
"v_exp_f32 v31, v31\n"
"v_exp_f32 v32, v32\n"
"v_exp_f32 v33, v33\n"
"v_smfmac_f32_32x32x32_bf16 v[100:115], v[152:155], v[24:31], v164\n"
"v_exp_f32 v34, v34\n"
"v_exp_f32 v35, v35\n"
"v_exp_f32 v36, v36\n"
"v_mfma_f32_32x32x16_bf16 v[16:31], v[144:147], v[132:135], v[48:63]\n"
"v_exp_f32 v37, v37\n"
"v_exp_f32 v38, v38\n"
"v_exp_f32 v39, v39\n"
"v_exp_f32 v40, v40\n"
"v_exp_f32 v41, v41\n"
"v_exp_f32 v42, v42\n"
"v_smfmac_f32_32x32x32_bf16 v[116:131], v[148:151], v[32:39], v164\n"
"ds_read_b128 v[140:143], v236 offset:6144\n"
"v_exp_f32 v43, v43\n"
"v_exp_f32 v44, v44\n"
"v_exp_f32 v45, v45\n"
"v_exp_f32 v46, v46\n"
"v_exp_f32 v47, v47\n"
"v_exp_f32 v16, v16\n"
"v_exp_f32 v17, v17\n"
"v_smfmac_f32_32x32x32_bf16 v[116:131], v[152:155], v[40:47], v164\n"
"s_waitcnt lgkmcnt(0)\n"
"v_exp_f32 v18, v18\n"
"v_exp_f32 v19, v19\n"
"v_exp_f32 v20, v20\n"
"v_mfma_f32_32x32x16_bf16 v[32:47], v[144:147], v[136:139], v[64:79]\n"
"v_exp_f32 v21, v21\n"
"v_exp_f32 v22, v22\n"
"v_exp_f32 v23, v23\n"
"v_exp_f32 v24, v24\n"
"v_exp_f32 v25, v25\n"
"v_exp_f32 v26, v26\n"
"v_smfmac_f32_32x32x32_bf16 v[100:115], v[156:159], v[16:23], v164\n"
"s_mov_b64 exec, s[90:91]\n"
"ds_read_b128 v[148:151], v238 offset:6144\n"
"ds_read_b128 v[152:155], v238 offset:6656\n"
"s_mov_b64 exec, -1\n"
"v_exp_f32 v27, v27\n"
"v_exp_f32 v28, v28\n"
"v_exp_f32 v29, v29\n"
"v_exp_f32 v30, v30\n"
"v_exp_f32 v31, v31\n"
"v_exp_f32 v32, v32\n"
"v_exp_f32 v33, v33\n"
"v_smfmac_f32_32x32x32_bf16 v[100:115], v[160:163], v[24:31], v164\n"
"v_exp_f32 v34, v34\n"
"v_exp_f32 v35, v35\n"
"v_exp_f32 v36, v36\n"
"v_mfma_f32_32x32x16_bf16 v[16:31], v[140:143], v[132:135], v[48:63]\n"
"v_exp_f32 v37, v37\n"
"v_exp_f32 v38, v38\n"
"v_exp_f32 v39, v39\n"
"v_exp_f32 v40, v40\n"
"v_exp_f32 v41, v41\n"
"v_exp_f32 v42, v42\n"
"v_smfmac_f32_32x32x32_bf16 v[116:131], v[156:159], v[32:39], v164\n"
"ds_read_b128 v[144:147], v236 offset:22528\n"
"v_exp_f32 v43, v43\n"
"v_exp_f32 v44, v44\n"
"v_exp_f32 v45, v45\n"
"v_exp_f32 v46, v46\n"
"v_exp_f32 v47, v47\n"
"v_exp_f32 v16, v16\n"
"v_exp_f32 v17, v17\n"
"v_smfmac_f32_32x32x32_bf16 v[116:131], v[160:163], v[40:47], v164\n"
"s_waitcnt lgkmcnt(0)\n"
"v_exp_f32 v18, v18\n"
"v_exp_f32 v19, v19\n"
"v_exp_f32 v20, v20\n"
"v_mfma_f32_32x32x16_bf16 v[32:47], v[140:143], v[136:139], v[64:79]\n"
"v_exp_f32 v21, v21\n"
"v_exp_f32 v22, v22\n"
"v_exp_f32 v23, v23\n"
"v_exp_f32 v24, v24\n"
"v_exp_f32 v25, v25\n"
"v_exp_f32 v26, v26\n"
"v_smfmac_f32_32x32x32_bf16 v[100:115], v[148:151], v[16:23], v164\n"
"s_mov_b64 exec, s[90:91]\n"
"ds_read_b128 v[156:159], v238 offset:22528\n"
"ds_read_b128 v[160:163], v238 offset:23040\n"
"s_mov_b64 exec, -1\n"
"v_exp_f32 v27, v27\n"
"v_exp_f32 v28, v28\n"
"v_exp_f32 v29, v29\n"
"v_exp_f32 v30, v30\n"
"v_exp_f32 v31, v31\n"
"v_exp_f32 v32, v32\n"
"v_exp_f32 v33, v33\n"
"v_smfmac_f32_32x32x32_bf16 v[100:115], v[152:155], v[24:31], v164\n"
"v_exp_f32 v34, v34\n"
"v_exp_f32 v35, v35\n"
"v_exp_f32 v36, v36\n"
"v_mfma_f32_32x32x16_bf16 v[16:31], v[144:147], v[132:135], v[48:63]\n"
"v_exp_f32 v37, v37\n"
"v_exp_f32 v38, v38\n"
"v_exp_f32 v39, v39\n"
"v_exp_f32 v40, v40\n"
"v_exp_f32 v41, v41\n"
"v_exp_f32 v42, v42\n"
"v_smfmac_f32_32x32x32_bf16 v[116:131], v[148:151], v[32:39], v164\n"
"ds_read_b128 v[140:143], v237 offset:6144\n"
"v_exp_f32 v43, v43\n"
"v_exp_f32 v44, v44\n"
"v_exp_f32 v45, v45\n"
"v_exp_f32 v46, v46\n"
"v_exp_f32 v47, v47\n"
"v_exp_f32 v16, v16\n"
"v_exp_f32 v17, v17\n"
"v_smfmac_f32_32x32x32_bf16 v[116:131], v[152:155], v[40:47], v164\n"
"s_waitcnt lgkmcnt(0)\n"
"v_exp_f32 v18, v18\n"
"v_exp_f32 v19, v19\n"
"v_exp_f32 v20, v20\n"
"v_mfma_f32_32x32x16_bf16 v[32:47], v[144:147], v[136:139], v[64:79]\n"
"v_exp_f32 v21, v21\n"
"v_exp_f32 v22, v22\n"
"v_exp_f32 v23, v23\n"
"v_exp_f32 v24, v24\n"
"v_exp_f32 v25, v25\n"
"v_exp_f32 v26, v26\n"
"v_smfmac_f32_32x32x32_bf16 v[100:115], v[156:159], v[16:23], v164\n"
"s_mov_b64 exec, s[90:91]\n"
"ds_read_b128 v[148:151], v239 offset:6144\n"
"ds_read_b128 v[152:155], v239 offset:6656\n"
"s_mov_b64 exec, -1\n"
"v_exp_f32 v27, v27\n"
"v_exp_f32 v28, v28\n"
"v_exp_f32 v29, v29\n"
"v_exp_f32 v30, v30\n"
"v_exp_f32 v31, v31\n"
"v_exp_f32 v32, v32\n"
"v_exp_f32 v33, v33\n"
"v_smfmac_f32_32x32x32_bf16 v[100:115], v[160:163], v[24:31], v164\n"
"v_exp_f32 v34, v34\n"
"v_exp_f32 v35, v35\n"
"v_exp_f32 v36, v36\n"
"v_mfma_f32_32x32x16_bf16 v[16:31], v[140:143], v[132:135], v[48:63]\n"
"v_exp_f32 v37, v37\n"
"v_exp_f32 v38, v38\n"
"v_exp_f32 v39, v39\n"
"v_exp_f32 v40, v40\n"
"v_exp_f32 v41, v41\n"
"v_exp_f32 v42, v42\n"
"v_smfmac_f32_32x32x32_bf16 v[116:131], v[156:159], v[32:39], v164\n"
"ds_read_b128 v[144:147], v237 offset:22528\n"
"v_exp_f32 v43, v43\n"
"v_exp_f32 v44, v44\n"
"v_exp_f32 v45, v45\n"
"v_exp_f32 v46, v46\n"
"v_exp_f32 v47, v47\n"
"v_exp_f32 v16, v16\n"
"v_exp_f32 v17, v17\n"
"v_smfmac_f32_32x32x32_bf16 v[116:131], v[160:163], v[40:47], v164\n"
"s_waitcnt lgkmcnt(0)\n"
"v_exp_f32 v18, v18\n"
"v_exp_f32 v19, v19\n"
"v_exp_f32 v20, v20\n"
"v_mfma_f32_32x32x16_bf16 v[32:47], v[140:143], v[136:139], v[64:79]\n"
"v_exp_f32 v21, v21\n"
"v_exp_f32 v22, v22\n"
"v_exp_f32 v23, v23\n"
"v_exp_f32 v24, v24\n"
"v_exp_f32 v25, v25\n"
"v_exp_f32 v26, v26\n"
"v_smfmac_f32_32x32x32_bf16 v[100:115], v[148:151], v[16:23], v164\n"
"s_mov_b64 exec, s[90:91]\n"
"ds_read_b128 v[156:159], v239 offset:22528\n"
"ds_read_b128 v[160:163], v239 offset:23040\n"
"s_mov_b64 exec, -1\n"
"v_exp_f32 v27, v27\n"
"v_exp_f32 v28, v28\n"
"v_exp_f32 v29, v29\n"
"v_exp_f32 v30, v30\n"
"v_exp_f32 v31, v31\n"
"v_exp_f32 v32, v32\n"
"v_exp_f32 v33, v33\n"
"v_smfmac_f32_32x32x32_bf16 v[100:115], v[152:155], v[24:31], v164\n"
"v_exp_f32 v34, v34\n"
"v_exp_f32 v35, v35\n"
"v_exp_f32 v36, v36\n"
"v_mfma_f32_32x32x16_bf16 v[16:31], v[144:147], v[132:135], v[48:63]\n"
"v_exp_f32 v37, v37\n"
"v_exp_f32 v38, v38\n"
"v_exp_f32 v39, v39\n"
"v_exp_f32 v40, v40\n"
"v_exp_f32 v41, v41\n"
"v_exp_f32 v42, v42\n"
"v_smfmac_f32_32x32x32_bf16 v[116:131], v[148:151], v[32:39], v164\n"
"ds_read_b128 v[140:143], v236 offset:8192\n"
"v_exp_f32 v43, v43\n"
"v_exp_f32 v44, v44\n"
"v_exp_f32 v45, v45\n"
"v_exp_f32 v46, v46\n"
"v_exp_f32 v47, v47\n"
"v_exp_f32 v16, v16\n"
"v_exp_f32 v17, v17\n"
"v_smfmac_f32_32x32x32_bf16 v[116:131], v[152:155], v[40:47], v164\n"
"s_waitcnt lgkmcnt(0)\n"
"v_exp_f32 v18, v18\n"
"v_exp_f32 v19, v19\n"
"v_exp_f32 v20, v20\n"
"v_mfma_f32_32x32x16_bf16 v[32:47], v[144:147], v[136:139], v[64:79]\n"
"v_exp_f32 v21, v21\n"
"v_exp_f32 v22, v22\n"
"v_exp_f32 v23, v23\n"
"v_exp_f32 v24, v24\n"
"v_exp_f32 v25, v25\n"
"v_exp_f32 v26, v26\n"
"v_smfmac_f32_32x32x32_bf16 v[100:115], v[156:159], v[16:23], v164\n"
"s_mov_b64 exec, s[90:91]\n"
"ds_read_b128 v[148:151], v238 offset:8192\n"
"ds_read_b128 v[152:155], v238 offset:8704\n"
"s_mov_b64 exec, -1\n"
"v_exp_f32 v27, v27\n"
"v_exp_f32 v28, v28\n"
"v_exp_f32 v29, v29\n"
"v_exp_f32 v30, v30\n"
"v_exp_f32 v31, v31\n"
"v_exp_f32 v32, v32\n"
"v_exp_f32 v33, v33\n"
"v_smfmac_f32_32x32x32_bf16 v[100:115], v[160:163], v[24:31], v164\n"
"v_exp_f32 v34, v34\n"
"v_exp_f32 v35, v35\n"
"v_exp_f32 v36, v36\n"
"v_mfma_f32_32x32x16_bf16 v[16:31], v[140:143], v[132:135], v[48:63]\n"
"v_exp_f32 v37, v37\n"
"v_exp_f32 v38, v38\n"
"v_exp_f32 v39, v39\n"
"v_exp_f32 v40, v40\n"
"v_exp_f32 v41, v41\n"
"v_exp_f32 v42, v42\n"
"v_smfmac_f32_32x32x32_bf16 v[116:131], v[156:159], v[32:39], v164\n"
"ds_read_b128 v[144:147], v236 offset:24576\n"
"v_exp_f32 v43, v43\n"
"v_exp_f32 v44, v44\n"
"v_exp_f32 v45, v45\n"
"v_exp_f32 v46, v46\n"
"v_exp_f32 v47, v47\n"
"v_exp_f32 v16, v16\n"
"v_exp_f32 v17, v17\n"
"v_smfmac_f32_32x32x32_bf16 v[116:131], v[160:163], v[40:47], v164\n"
"s_waitcnt lgkmcnt(0)\n"
"v_exp_f32 v18, v18\n"
"v_exp_f32 v19, v19\n"
"v_exp_f32 v20, v20\n"
"v_mfma_f32_32x32x16_bf16 v[32:47], v[140:143], v[136:139], v[64:79]\n"
"v_exp_f32 v21, v21\n"
"v_exp_f32 v22, v22\n"
"v_exp_f32 v23, v23\n"
"v_exp_f32 v24, v24\n"
"v_exp_f32 v25, v25\n"
"v_exp_f32 v26, v26\n"
"v_smfmac_f32_32x32x32_bf16 v[100:115], v[148:151], v[16:23], v164\n"
"s_mov_b64 exec, s[90:91]\n"
"ds_read_b128 v[156:159], v238 offset:24576\n"
"ds_read_b128 v[160:163], v238 offset:25088\n"
"s_mov_b64 exec, -1\n"
"v_exp_f32 v27, v27\n"
"v_exp_f32 v28, v28\n"
"v_exp_f32 v29, v29\n"
"v_exp_f32 v30, v30\n"
"v_exp_f32 v31, v31\n"
"v_exp_f32 v32, v32\n"
"v_exp_f32 v33, v33\n"
"v_smfmac_f32_32x32x32_bf16 v[100:115], v[152:155], v[24:31], v164\n"
"v_exp_f32 v34, v34\n"
"v_exp_f32 v35, v35\n"
"v_exp_f32 v36, v36\n"
"v_mfma_f32_32x32x16_bf16 v[16:31], v[144:147], v[132:135], v[48:63]\n"
"v_exp_f32 v37, v37\n"
"v_exp_f32 v38, v38\n"
"v_exp_f32 v39, v39\n"
"v_exp_f32 v40, v40\n"
"v_exp_f32 v41, v41\n"
"v_exp_f32 v42, v42\n"
"v_smfmac_f32_32x32x32_bf16 v[116:131], v[148:151], v[32:39], v164\n"
"ds_read_b128 v[140:143], v237 offset:8192\n"
"v_exp_f32 v43, v43\n"
"v_exp_f32 v44, v44\n"
"v_exp_f32 v45, v45\n"
"v_exp_f32 v46, v46\n"
"v_exp_f32 v47, v47\n"
"v_exp_f32 v16, v16\n"
"v_exp_f32 v17, v17\n"
"v_smfmac_f32_32x32x32_bf16 v[116:131], v[152:155], v[40:47], v164\n"
"s_waitcnt lgkmcnt(0)\n"
"v_exp_f32 v18, v18\n"
"v_exp_f32 v19, v19\n"
"v_exp_f32 v20, v20\n"
"v_mfma_f32_32x32x16_bf16 v[32:47], v[144:147], v[136:139], v[64:79]\n"
"v_exp_f32 v21, v21\n"
"v_exp_f32 v22, v22\n"
"v_exp_f32 v23, v23\n"
"v_exp_f32 v24, v24\n"
"v_exp_f32 v25, v25\n"
"v_exp_f32 v26, v26\n"
"v_smfmac_f32_32x32x32_bf16 v[100:115], v[156:159], v[16:23], v164\n"
"s_mov_b64 exec, s[90:91]\n"
"ds_read_b128 v[148:151], v239 offset:8192\n"
"ds_read_b128 v[152:155], v239 offset:8704\n"
"s_mov_b64 exec, -1\n"
"v_exp_f32 v27, v27\n"
"v_exp_f32 v28, v28\n"
"v_exp_f32 v29, v29\n"
"v_exp_f32 v30, v30\n"
"v_exp_f32 v31, v31\n"
"v_exp_f32 v32, v32\n"
"v_exp_f32 v33, v33\n"
"v_smfmac_f32_32x32x32_bf16 v[100:115], v[160:163], v[24:31], v164\n"
"v_exp_f32 v34, v34\n"
"v_exp_f32 v35, v35\n"
"v_exp_f32 v36, v36\n"
"v_mfma_f32_32x32x16_bf16 v[16:31], v[140:143], v[132:135], v[48:63]\n"
"v_exp_f32 v37, v37\n"
"v_exp_f32 v38, v38\n"
"v_exp_f32 v39, v39\n"
"v_exp_f32 v40, v40\n"
"v_exp_f32 v41, v41\n"
"v_exp_f32 v42, v42\n"
"v_smfmac_f32_32x32x32_bf16 v[116:131], v[156:159], v[32:39], v164\n"
"ds_read_b128 v[144:147], v237 offset:24576\n"
"v_exp_f32 v43, v43\n"
"v_exp_f32 v44, v44\n"
"v_exp_f32 v45, v45\n"
"v_exp_f32 v46, v46\n"
"v_exp_f32 v47, v47\n"
"v_exp_f32 v16, v16\n"
"v_exp_f32 v17, v17\n"
"v_smfmac_f32_32x32x32_bf16 v[116:131], v[160:163], v[40:47], v164\n"
"s_waitcnt lgkmcnt(0)\n"
"v_exp_f32 v18, v18\n"
"v_exp_f32 v19, v19\n"
"v_exp_f32 v20, v20\n"
"v_mfma_f32_32x32x16_bf16 v[32:47], v[140:143], v[136:139], v[64:79]\n"
"v_exp_f32 v21, v21\n"
"v_exp_f32 v22, v22\n"
"v_exp_f32 v23, v23\n"
"v_exp_f32 v24, v24\n"
"v_exp_f32 v25, v25\n"
"v_exp_f32 v26, v26\n"
"v_smfmac_f32_32x32x32_bf16 v[100:115], v[148:151], v[16:23], v164\n"
"s_mov_b64 exec, s[90:91]\n"
"ds_read_b128 v[156:159], v239 offset:24576\n"
"ds_read_b128 v[160:163], v239 offset:25088\n"
"s_mov_b64 exec, -1\n"
"v_exp_f32 v27, v27\n"
"v_exp_f32 v28, v28\n"
"v_exp_f32 v29, v29\n"
"v_exp_f32 v30, v30\n"
"v_exp_f32 v31, v31\n"
"v_exp_f32 v32, v32\n"
"v_exp_f32 v33, v33\n"
"v_smfmac_f32_32x32x32_bf16 v[100:115], v[152:155], v[24:31], v164\n"
"v_exp_f32 v34, v34\n"
"v_exp_f32 v35, v35\n"
"v_exp_f32 v36, v36\n"
"v_mfma_f32_32x32x16_bf16 v[16:31], v[144:147], v[132:135], v[48:63]\n"
"v_exp_f32 v37, v37\n"
"v_exp_f32 v38, v38\n"
"v_exp_f32 v39, v39\n"
"v_exp_f32 v40, v40\n"
"v_exp_f32 v41, v41\n"
"v_exp_f32 v42, v42\n"
"v_smfmac_f32_32x32x32_bf16 v[116:131], v[148:151], v[32:39], v164\n"
"ds_read_b128 v[140:143], v236 offset:10240\n"
"v_exp_f32 v43, v43\n"
"v_exp_f32 v44, v44\n"
"v_exp_f32 v45, v45\n"
"v_exp_f32 v46, v46\n"
"v_exp_f32 v47, v47\n"
"v_exp_f32 v16, v16\n"
"v_exp_f32 v17, v17\n"
"v_smfmac_f32_32x32x32_bf16 v[116:131], v[152:155], v[40:47], v164\n"
"s_waitcnt lgkmcnt(0)\n"
"v_exp_f32 v18, v18\n"
"v_exp_f32 v19, v19\n"
"v_exp_f32 v20, v20\n"
"v_mfma_f32_32x32x16_bf16 v[32:47], v[144:147], v[136:139], v[64:79]\n"
"v_exp_f32 v21, v21\n"
"v_exp_f32 v22, v22\n"
"v_exp_f32 v23, v23\n"
"v_exp_f32 v24, v24\n"
"v_exp_f32 v25, v25\n"
"v_exp_f32 v26, v26\n"
"v_smfmac_f32_32x32x32_bf16 v[100:115], v[156:159], v[16:23], v164\n"
"s_mov_b64 exec, s[90:91]\n"
"ds_read_b128 v[148:151], v238 offset:10240\n"
"ds_read_b128 v[152:155], v238 offset:10752\n"
"s_mov_b64 exec, -1\n"
"v_exp_f32 v27, v27\n"
"v_exp_f32 v28, v28\n"
"v_exp_f32 v29, v29\n"
"v_exp_f32 v30, v30\n"
"v_exp_f32 v31, v31\n"
"v_exp_f32 v32, v32\n"
"v_exp_f32 v33, v33\n"
"v_smfmac_f32_32x32x32_bf16 v[100:115], v[160:163], v[24:31], v164\n"
"s_cmp_ge_u32 s68, 4\n"
"s_cbranch_scc1 Lcls_111_%=\n"
"s_barrier\n"
"Lcls_111_%=:\n"
"v_exp_f32 v34, v34\n"
"v_exp_f32 v35, v35\n"
"v_exp_f32 v36, v36\n"
"v_mfma_f32_32x32x16_bf16 v[16:31], v[140:143], v[132:135], v[48:63]\n"
"v_exp_f32 v37, v37\n"
"v_exp_f32 v38, v38\n"
"v_exp_f32 v39, v39\n"
"v_exp_f32 v40, v40\n"
"v_exp_f32 v41, v41\n"
"v_exp_f32 v42, v42\n"
"v_smfmac_f32_32x32x32_bf16 v[116:131], v[156:159], v[32:39], v164\n"
"ds_read_b128 v[144:147], v236 offset:26624\n"
"v_exp_f32 v43, v43\n"
"v_exp_f32 v44, v44\n"
"v_exp_f32 v45, v45\n"
"v_exp_f32 v46, v46\n"
"v_exp_f32 v47, v47\n"
"v_exp_f32 v16, v16\n"
"v_exp_f32 v17, v17\n"
"v_smfmac_f32_32x32x32_bf16 v[116:131], v[160:163], v[40:47], v164\n"
"s_waitcnt lgkmcnt(0)\n"
"v_exp_f32 v18, v18\n"
"v_exp_f32 v19, v19\n"
"v_exp_f32 v20, v20\n"
"v_mfma_f32_32x32x16_bf16 v[32:47], v[140:143], v[136:139], v[64:79]\n"
"v_exp_f32 v21, v21\n"
"v_exp_f32 v22, v22\n"
"v_exp_f32 v23, v23\n"
"v_exp_f32 v24, v24\n"
"v_exp_f32 v25, v25\n"
"v_exp_f32 v26, v26\n"
"v_smfmac_f32_32x32x32_bf16 v[100:115], v[148:151], v[16:23], v164\n"
"s_mov_b64 exec, s[90:91]\n"
"ds_read_b128 v[156:159], v238 offset:26624\n"
"ds_read_b128 v[160:163], v238 offset:27136\n"
"s_mov_b64 exec, -1\n"
"v_exp_f32 v27, v27\n"
"v_exp_f32 v28, v28\n"
"v_exp_f32 v29, v29\n"
"v_exp_f32 v30, v30\n"
"v_exp_f32 v31, v31\n"
"v_exp_f32 v32, v32\n"
"v_exp_f32 v33, v33\n"
"v_smfmac_f32_32x32x32_bf16 v[100:115], v[152:155], v[24:31], v164\n"
"s_cmp_ge_u32 s68, 4\n"
"s_cbranch_scc0 Lcls_112_%=\n"
"buffer_load_dwordx2 v[188:189], v247, s[32:35], s87 offen\n"
"buffer_load_dwordx2 v[190:191], v247, s[32:35], s87 offen offset:16\n"
"buffer_load_dwordx2 v[192:193], v248, s[60:63], s97 offen\n"
"buffer_load_dwordx2 v[194:195], v248, s[60:63], s97 offen offset:16\n"
"Lcls_112_%=:\n"
"v_exp_f32 v34, v34\n"
"v_exp_f32 v35, v35\n"
"v_exp_f32 v36, v36\n"
"v_mfma_f32_32x32x16_bf16 v[16:31], v[144:147], v[132:135], v[48:63]\n"
"v_exp_f32 v37, v37\n"
"v_exp_f32 v38, v38\n"
"v_exp_f32 v39, v39\n"
"v_exp_f32 v40, v40\n"
"v_exp_f32 v41, v41\n"
"v_exp_f32 v42, v42\n"
"v_smfmac_f32_32x32x32_bf16 v[116:131], v[148:151], v[32:39], v164\n"
"ds_read_b128 v[140:143], v237 offset:10240\n"
"v_exp_f32 v43, v43\n"
"v_exp_f32 v44, v44\n"
"v_exp_f32 v45, v45\n"
"v_exp_f32 v46, v46\n"
"v_exp_f32 v47, v47\n"
"v_exp_f32 v16, v16\n"
"v_exp_f32 v17, v17\n"
"v_smfmac_f32_32x32x32_bf16 v[116:131], v[152:155], v[40:47], v164\n"
"s_waitcnt lgkmcnt(0)\n"
"v_exp_f32 v18, v18\n"
"v_exp_f32 v19, v19\n"
"v_exp_f32 v20, v20\n"
"v_mfma_f32_32x32x16_bf16 v[32:47], v[144:147], v[136:139], v[64:79]\n"
"v_exp_f32 v21, v21\n"
"v_exp_f32 v22, v22\n"
"v_exp_f32 v23, v23\n"
"v_exp_f32 v24, v24\n"
"v_exp_f32 v25, v25\n"
"v_exp_f32 v26, v26\n"
"v_smfmac_f32_32x32x32_bf16 v[100:115], v[156:159], v[16:23], v164\n"
"s_mov_b64 exec, s[90:91]\n"
"ds_read_b128 v[148:151], v239 offset:10240\n"
"ds_read_b128 v[152:155], v239 offset:10752\n"
"s_mov_b64 exec, -1\n"
"v_exp_f32 v27, v27\n"
"v_exp_f32 v28, v28\n"
"v_exp_f32 v29, v29\n"
"v_exp_f32 v30, v30\n"
"v_exp_f32 v31, v31\n"
"v_exp_f32 v32, v32\n"
"v_exp_f32 v33, v33\n"
"v_smfmac_f32_32x32x32_bf16 v[100:115], v[160:163], v[24:31], v164\n"
"v_exp_f32 v34, v34\n"
"v_exp_f32 v35, v35\n"
"v_exp_f32 v36, v36\n"
"v_mfma_f32_32x32x16_bf16 v[16:31], v[140:143], v[132:135], v[48:63]\n"
"v_exp_f32 v37, v37\n"
"v_exp_f32 v38, v38\n"
"v_exp_f32 v39, v39\n"
"v_exp_f32 v40, v40\n"
"v_exp_f32 v41, v41\n"
"v_exp_f32 v42, v42\n"
"v_smfmac_f32_32x32x32_bf16 v[116:131], v[156:159], v[32:39], v164\n"
"ds_read_b128 v[144:147], v237 offset:26624\n"
"v_exp_f32 v43, v43\n"
"v_exp_f32 v44, v44\n"
"v_exp_f32 v45, v45\n"
"v_exp_f32 v46, v46\n"
"v_exp_f32 v47, v47\n"
"v_exp_f32 v16, v16\n"
"v_exp_f32 v17, v17\n"
"v_smfmac_f32_32x32x32_bf16 v[116:131], v[160:163], v[40:47], v164\n"
"s_waitcnt lgkmcnt(0)\n"
"v_exp_f32 v18, v18\n"
"v_exp_f32 v19, v19\n"
"v_exp_f32 v20, v20\n"
"v_mfma_f32_32x32x16_bf16 v[32:47], v[140:143], v[136:139], v[64:79]\n"
"v_exp_f32 v21, v21\n"
"v_exp_f32 v22, v22\n"
"v_exp_f32 v23, v23\n"
"v_exp_f32 v24, v24\n"
"v_exp_f32 v25, v25\n"
"v_exp_f32 v26, v26\n"
"v_smfmac_f32_32x32x32_bf16 v[100:115], v[148:151], v[16:23], v164\n"
"s_mov_b64 exec, s[90:91]\n"
"ds_read_b128 v[156:159], v239 offset:26624\n"
"ds_read_b128 v[160:163], v239 offset:27136\n"
"s_mov_b64 exec, -1\n"
"v_exp_f32 v27, v27\n"
"v_exp_f32 v28, v28\n"
"v_exp_f32 v29, v29\n"
"v_exp_f32 v30, v30\n"
"v_exp_f32 v31, v31\n"
"v_exp_f32 v32, v32\n"
"v_exp_f32 v33, v33\n"
"v_smfmac_f32_32x32x32_bf16 v[100:115], v[152:155], v[24:31], v164\n"
"v_exp_f32 v34, v34\n"
"v_exp_f32 v35, v35\n"
"v_exp_f32 v36, v36\n"
"v_mfma_f32_32x32x16_bf16 v[16:31], v[144:147], v[132:135], v[48:63]\n"
"v_exp_f32 v37, v37\n"
"v_exp_f32 v38, v38\n"
"v_exp_f32 v39, v39\n"
"v_exp_f32 v40, v40\n"
"v_exp_f32 v41, v41\n"
"v_exp_f32 v42, v42\n"
"v_smfmac_f32_32x32x32_bf16 v[116:131], v[148:151], v[32:39], v164\n"
"ds_read_b128 v[140:143], v236 offset:12288\n"
"v_exp_f32 v43, v43\n"
"v_exp_f32 v44, v44\n"
"v_exp_f32 v45, v45\n"
"v_exp_f32 v46, v46\n"
"v_exp_f32 v47, v47\n"
"v_exp_f32 v16, v16\n"
"v_exp_f32 v17, v17\n"
"v_smfmac_f32_32x32x32_bf16 v[116:131], v[152:155], v[40:47], v164\n"
"s_waitcnt lgkmcnt(0)\n"
"v_exp_f32 v18, v18\n"
"v_exp_f32 v19, v19\n"
"v_exp_f32 v20, v20\n"
"v_mfma_f32_32x32x16_bf16 v[32:47], v[144:147], v[136:139], v[64:79]\n"
"v_exp_f32 v21, v21\n"
"v_exp_f32 v22, v22\n"
"v_exp_f32 v23, v23\n"
"v_exp_f32 v24, v24\n"
"v_exp_f32 v25, v25\n"
"v_exp_f32 v26, v26\n"
"v_smfmac_f32_32x32x32_bf16 v[100:115], v[156:159], v[16:23], v164\n"
"s_mov_b64 exec, s[90:91]\n"
"ds_read_b128 v[148:151], v238 offset:12288\n"
"ds_read_b128 v[152:155], v238 offset:12800\n"
"s_mov_b64 exec, -1\n"
"v_exp_f32 v27, v27\n"
"v_exp_f32 v28, v28\n"
"v_exp_f32 v29, v29\n"
"v_exp_f32 v30, v30\n"
"v_exp_f32 v31, v31\n"
"v_exp_f32 v32, v32\n"
"v_exp_f32 v33, v33\n"
"v_smfmac_f32_32x32x32_bf16 v[100:115], v[160:163], v[24:31], v164\n"
"v_exp_f32 v34, v34\n"
"v_exp_f32 v35, v35\n"
"v_exp_f32 v36, v36\n"
"v_mfma_f32_32x32x16_bf16 v[16:31], v[140:143], v[132:135], v[48:63]\n"
"v_exp_f32 v37, v37\n"
"v_exp_f32 v38, v38\n"
"v_exp_f32 v39, v39\n"
"v_exp_f32 v40, v40\n"
"v_exp_f32 v41, v41\n"
"v_exp_f32 v42, v42\n"
"v_smfmac_f32_32x32x32_bf16 v[116:131], v[156:159], v[32:39], v164\n"
"ds_read_b128 v[144:147], v236 offset:14336\n"
"v_exp_f32 v43, v43\n"
"v_exp_f32 v44, v44\n"
"v_exp_f32 v45, v45\n"
"v_exp_f32 v46, v46\n"
"v_exp_f32 v47, v47\n"
"v_exp_f32 v16, v16\n"
"v_exp_f32 v17, v17\n"
"v_smfmac_f32_32x32x32_bf16 v[116:131], v[160:163], v[40:47], v164\n"
"s_waitcnt lgkmcnt(0)\n"
"v_exp_f32 v18, v18\n"
"v_exp_f32 v19, v19\n"
"v_exp_f32 v20, v20\n"
"v_mfma_f32_32x32x16_bf16 v[32:47], v[140:143], v[136:139], v[64:79]\n"
"v_exp_f32 v21, v21\n"
"v_exp_f32 v22, v22\n"
"v_exp_f32 v23, v23\n"
"v_exp_f32 v24, v24\n"
"v_exp_f32 v25, v25\n"
"v_exp_f32 v26, v26\n"
"v_smfmac_f32_32x32x32_bf16 v[100:115], v[148:151], v[16:23], v164\n"
"s_mov_b64 exec, s[90:91]\n"
"ds_read_b128 v[156:159], v238 offset:14336\n"
"ds_read_b128 v[160:163], v238 offset:14848\n"
"s_mov_b64 exec, -1\n"
"v_exp_f32 v27, v27\n"
"v_exp_f32 v28, v28\n"
"v_exp_f32 v29, v29\n"
"v_exp_f32 v30, v30\n"
"v_exp_f32 v31, v31\n"
"v_exp_f32 v32, v32\n"
"v_exp_f32 v33, v33\n"
"v_smfmac_f32_32x32x32_bf16 v[100:115], v[152:155], v[24:31], v164\n"
"v_exp_f32 v34, v34\n"
"v_exp_f32 v35, v35\n"
"v_exp_f32 v36, v36\n"
"v_mfma_f32_32x32x16_bf16 v[16:31], v[144:147], v[132:135], v[48:63]\n"
"v_exp_f32 v37, v37\n"
"v_exp_f32 v38, v38\n"
"v_exp_f32 v39, v39\n"
"v_exp_f32 v40, v40\n"
"v_exp_f32 v41, v41\n"
"v_exp_f32 v42, v42\n"
"v_smfmac_f32_32x32x32_bf16 v[116:131], v[148:151], v[32:39], v164\n"
"ds_read_b128 v[140:143], v236 offset:28672\n"
"v_exp_f32 v43, v43\n"
"v_exp_f32 v44, v44\n"
"v_exp_f32 v45, v45\n"
"v_exp_f32 v46, v46\n"
"v_exp_f32 v47, v47\n"
"v_exp_f32 v16, v16\n"
"v_exp_f32 v17, v17\n"
"v_smfmac_f32_32x32x32_bf16 v[116:131], v[152:155], v[40:47], v164\n"
"s_waitcnt lgkmcnt(0)\n"
"v_exp_f32 v18, v18\n"
"v_exp_f32 v19, v19\n"
"v_exp_f32 v20, v20\n"
"v_mfma_f32_32x32x16_bf16 v[32:47], v[144:147], v[136:139], v[64:79]\n"
"v_exp_f32 v21, v21\n"
"v_exp_f32 v22, v22\n"
"v_exp_f32 v23, v23\n"
"v_exp_f32 v24, v24\n"
"v_exp_f32 v25, v25\n"
"v_exp_f32 v26, v26\n"
"v_smfmac_f32_32x32x32_bf16 v[100:115], v[156:159], v[16:23], v164\n"
"s_mov_b64 exec, s[90:91]\n"
"ds_read_b128 v[148:151], v238 offset:28672\n"
"ds_read_b128 v[152:155], v238 offset:29184\n"
"s_mov_b64 exec, -1\n"
"v_exp_f32 v27, v27\n"
"v_exp_f32 v28, v28\n"
"v_exp_f32 v29, v29\n"
"v_exp_f32 v30, v30\n"
"v_exp_f32 v31, v31\n"
"v_exp_f32 v32, v32\n"
"v_exp_f32 v33, v33\n"
"v_smfmac_f32_32x32x32_bf16 v[100:115], v[160:163], v[24:31], v164\n"
"s_cmp_ge_u32 s68, 4\n"
"s_cbranch_scc1 Lcls_113_%=\n"
"s_barrier\n"
"Lcls_113_%=:\n"
"v_exp_f32 v34, v34\n"
"v_exp_f32 v35, v35\n"
"v_exp_f32 v36, v36\n"
"v_mfma_f32_32x32x16_bf16 v[16:31], v[140:143], v[132:135], v[48:63]\n"
"v_exp_f32 v37, v37\n"
"v_exp_f32 v38, v38\n"
"v_exp_f32 v39, v39\n"
"v_exp_f32 v40, v40\n"
"v_exp_f32 v41, v41\n"
"v_exp_f32 v42, v42\n"
"v_smfmac_f32_32x32x32_bf16 v[116:131], v[156:159], v[32:39], v164\n"
"ds_read_b128 v[144:147], v236 offset:30720\n"
"v_exp_f32 v43, v43\n"
"v_exp_f32 v44, v44\n"
"v_exp_f32 v45, v45\n"
"v_exp_f32 v46, v46\n"
"v_exp_f32 v47, v47\n"
"v_exp_f32 v16, v16\n"
"v_exp_f32 v17, v17\n"
"v_smfmac_f32_32x32x32_bf16 v[116:131], v[160:163], v[40:47], v164\n"
"s_waitcnt lgkmcnt(0)\n"
"v_exp_f32 v18, v18\n"
"v_exp_f32 v19, v19\n"
"v_exp_f32 v20, v20\n"
"v_mfma_f32_32x32x16_bf16 v[32:47], v[140:143], v[136:139], v[64:79]\n"
"v_exp_f32 v21, v21\n"
"v_exp_f32 v22, v22\n"
"v_exp_f32 v23, v23\n"
"v_exp_f32 v24, v24\n"
"v_exp_f32 v25, v25\n"
"v_exp_f32 v26, v26\n"
"v_smfmac_f32_32x32x32_bf16 v[100:115], v[148:151], v[16:23], v164\n"
"s_mov_b64 exec, s[90:91]\n"
"ds_read_b128 v[156:159], v238 offset:30720\n"
"ds_read_b128 v[160:163], v238 offset:31232\n"
"s_mov_b64 exec, -1\n"
"v_exp_f32 v27, v27\n"
"v_exp_f32 v28, v28\n"
"v_exp_f32 v29, v29\n"
"v_exp_f32 v30, v30\n"
"v_exp_f32 v31, v31\n"
"v_exp_f32 v32, v32\n"
"v_exp_f32 v33, v33\n"
"v_smfmac_f32_32x32x32_bf16 v[100:115], v[152:155], v[24:31], v164\n"
"v_exp_f32 v34, v34\n"
"v_exp_f32 v35, v35\n"
"v_exp_f32 v36, v36\n"
"v_mfma_f32_32x32x16_bf16 v[16:31], v[144:147], v[132:135], v[48:63]\n"
"v_exp_f32 v37, v37\n"
"v_exp_f32 v38, v38\n"
"v_exp_f32 v39, v39\n"
"v_exp_f32 v40, v40\n"
"v_exp_f32 v41, v41\n"
"v_exp_f32 v42, v42\n"
"v_smfmac_f32_32x32x32_bf16 v[116:131], v[148:151], v[32:39], v164\n"
"ds_read_b128 v[140:143], v237 offset:12288\n"
"v_exp_f32 v43, v43\n"
"v_exp_f32 v44, v44\n"
"v_exp_f32 v45, v45\n"
"v_exp_f32 v46, v46\n"
"v_exp_f32 v47, v47\n"
"v_exp_f32 v16, v16\n"
"v_exp_f32 v17, v17\n"
"v_smfmac_f32_32x32x32_bf16 v[116:131], v[152:155], v[40:47], v164\n"
"s_waitcnt lgkmcnt(0)\n"
"v_exp_f32 v18, v18\n"
"v_exp_f32 v19, v19\n"
"v_exp_f32 v20, v20\n"
"v_mfma_f32_32x32x16_bf16 v[32:47], v[144:147], v[136:139], v[64:79]\n"
"v_exp_f32 v21, v21\n"
"v_exp_f32 v22, v22\n"
"v_exp_f32 v23, v23\n"
"v_exp_f32 v24, v24\n"
"v_exp_f32 v25, v25\n"
"v_exp_f32 v26, v26\n"
"v_smfmac_f32_32x32x32_bf16 v[100:115], v[156:159], v[16:23], v164\n"
"s_mov_b64 exec, s[90:91]\n"
"ds_read_b128 v[148:151], v239 offset:12288\n"
"ds_read_b128 v[152:155], v239 offset:12800\n"
"s_mov_b64 exec, -1\n"
"v_exp_f32 v27, v27\n"
"v_exp_f32 v28, v28\n"
"v_exp_f32 v29, v29\n"
"v_exp_f32 v30, v30\n"
"v_exp_f32 v31, v31\n"
"v_exp_f32 v32, v32\n"
"v_exp_f32 v33, v33\n"
"v_smfmac_f32_32x32x32_bf16 v[100:115], v[160:163], v[24:31], v164\n"
"v_exp_f32 v34, v34\n"
"v_exp_f32 v35, v35\n"
"v_exp_f32 v36, v36\n"
"v_mfma_f32_32x32x16_bf16 v[16:31], v[140:143], v[132:135], v[48:63]\n"
"v_exp_f32 v37, v37\n"
"v_exp_f32 v38, v38\n"
"v_exp_f32 v39, v39\n"
"v_exp_f32 v40, v40\n"
"v_exp_f32 v41, v41\n"
"v_exp_f32 v42, v42\n"
"v_smfmac_f32_32x32x32_bf16 v[116:131], v[156:159], v[32:39], v164\n"
"ds_read_b128 v[144:147], v237 offset:14336\n"
"v_exp_f32 v43, v43\n"
"v_exp_f32 v44, v44\n"
"v_exp_f32 v45, v45\n"
"v_exp_f32 v46, v46\n"
"v_exp_f32 v47, v47\n"
"v_exp_f32 v16, v16\n"
"v_exp_f32 v17, v17\n"
"v_smfmac_f32_32x32x32_bf16 v[116:131], v[160:163], v[40:47], v164\n"
"s_waitcnt lgkmcnt(0)\n"
"v_exp_f32 v18, v18\n"
"v_exp_f32 v19, v19\n"
"v_exp_f32 v20, v20\n"
"v_mfma_f32_32x32x16_bf16 v[32:47], v[140:143], v[136:139], v[64:79]\n"
"v_exp_f32 v21, v21\n"
"v_exp_f32 v22, v22\n"
"v_exp_f32 v23, v23\n"
"v_exp_f32 v24, v24\n"
"v_exp_f32 v25, v25\n"
"v_exp_f32 v26, v26\n"
"v_smfmac_f32_32x32x32_bf16 v[100:115], v[148:151], v[16:23], v164\n"
"s_mov_b64 exec, s[90:91]\n"
"ds_read_b128 v[156:159], v239 offset:14336\n"
"ds_read_b128 v[160:163], v239 offset:14848\n"
"s_mov_b64 exec, -1\n"
"v_exp_f32 v27, v27\n"
"v_exp_f32 v28, v28\n"
"v_exp_f32 v29, v29\n"
"v_exp_f32 v30, v30\n"
"v_exp_f32 v31, v31\n"
"v_exp_f32 v32, v32\n"
"v_exp_f32 v33, v33\n"
"v_smfmac_f32_32x32x32_bf16 v[100:115], v[152:155], v[24:31], v164\n"
"s_cmp_ge_u32 s68, 4\n"
"s_cbranch_scc0 Lcls_114_%=\n"
"ds_read_b128 v[168:171], v242 offset:0\n"
"ds_read_b128 v[172:175], v242 offset:1024\n"
"ds_read_b128 v[176:179], v242 offset:2048\n"
"ds_read_b128 v[180:183], v242 offset:3072\n"
"ds_read2st64_b32 v[184:185], v229 offset0:0 offset1:1\n"
"Lcls_114_%=:\n"
"v_exp_f32 v34, v34\n"
"v_exp_f32 v35, v35\n"
"v_exp_f32 v36, v36\n"
"v_mfma_f32_32x32x16_bf16 v[16:31], v[144:147], v[132:135], v[48:63]\n"
"v_exp_f32 v37, v37\n"
"v_exp_f32 v38, v38\n"
"v_exp_f32 v39, v39\n"
"v_exp_f32 v40, v40\n"
"v_exp_f32 v41, v41\n"
"v_exp_f32 v42, v42\n"
"v_smfmac_f32_32x32x32_bf16 v[116:131], v[148:151], v[32:39], v164\n"
"ds_read_b128 v[140:143], v237 offset:28672\n"
"v_exp_f32 v43, v43\n"
"v_exp_f32 v44, v44\n"
"v_exp_f32 v45, v45\n"
"v_exp_f32 v46, v46\n"
"v_exp_f32 v47, v47\n"
"v_exp_f32 v16, v16\n"
"v_exp_f32 v17, v17\n"
"v_smfmac_f32_32x32x32_bf16 v[116:131], v[152:155], v[40:47], v164\n"
"s_waitcnt lgkmcnt(0)\n"
"v_exp_f32 v18, v18\n"
"v_exp_f32 v19, v19\n"
"v_exp_f32 v20, v20\n"
"v_mfma_f32_32x32x16_bf16 v[32:47], v[144:147], v[136:139], v[64:79]\n"
"v_exp_f32 v21, v21\n"
"v_exp_f32 v22, v22\n"
"v_exp_f32 v23, v23\n"
"v_exp_f32 v24, v24\n"
"v_exp_f32 v25, v25\n"
"v_exp_f32 v26, v26\n"
"v_smfmac_f32_32x32x32_bf16 v[100:115], v[156:159], v[16:23], v164\n"
"s_mov_b64 exec, s[90:91]\n"
"ds_read_b128 v[148:151], v239 offset:28672\n"
"ds_read_b128 v[152:155], v239 offset:29184\n"
"s_mov_b64 exec, -1\n"
"v_exp_f32 v27, v27\n"
"v_exp_f32 v28, v28\n"
"v_exp_f32 v29, v29\n"
"v_exp_f32 v30, v30\n"
"v_exp_f32 v31, v31\n"
"v_exp_f32 v32, v32\n"
"v_exp_f32 v33, v33\n"
"v_smfmac_f32_32x32x32_bf16 v[100:115], v[160:163], v[24:31], v164\n"
"v_exp_f32 v34, v34\n"
"v_exp_f32 v35, v35\n"
"v_exp_f32 v36, v36\n"
"v_mfma_f32_32x32x16_bf16 v[16:31], v[140:143], v[132:135], v[48:63]\n"
"v_exp_f32 v37, v37\n"
"v_exp_f32 v38, v38\n"
"v_exp_f32 v39, v39\n"
"v_exp_f32 v40, v40\n"
"v_exp_f32 v41, v41\n"
"v_exp_f32 v42, v42\n"
"v_smfmac_f32_32x32x32_bf16 v[116:131], v[156:159], v[32:39], v164\n"
"ds_read_b128 v[144:147], v237 offset:30720\n"
"v_exp_f32 v43, v43\n"
"v_exp_f32 v44, v44\n"
"v_exp_f32 v45, v45\n"
"v_exp_f32 v46, v46\n"
"v_exp_f32 v47, v47\n"
"v_exp_f32 v16, v16\n"
"v_exp_f32 v17, v17\n"
"v_smfmac_f32_32x32x32_bf16 v[116:131], v[160:163], v[40:47], v164\n"
"s_waitcnt lgkmcnt(0)\n"
"v_exp_f32 v18, v18\n"
"v_exp_f32 v19, v19\n"
"v_exp_f32 v20, v20\n"
"v_mfma_f32_32x32x16_bf16 v[32:47], v[140:143], v[136:139], v[64:79]\n"
"v_exp_f32 v21, v21\n"
"v_exp_f32 v22, v22\n"
"v_exp_f32 v23, v23\n"
"v_exp_f32 v24, v24\n"
"v_exp_f32 v25, v25\n"
"v_exp_f32 v26, v26\n"
"v_smfmac_f32_32x32x32_bf16 v[100:115], v[148:151], v[16:23], v164\n"
"s_mov_b64 exec, s[90:91]\n"
"ds_read_b128 v[156:159], v239 offset:30720\n"
"ds_read_b128 v[160:163], v239 offset:31232\n"
"s_mov_b64 exec, -1\n"
"v_exp_f32 v27, v27\n"
"v_exp_f32 v28, v28\n"
"v_exp_f32 v29, v29\n"
"v_exp_f32 v30, v30\n"
"v_exp_f32 v31, v31\n"
"v_exp_f32 v32, v32\n"
"v_exp_f32 v33, v33\n"
"v_smfmac_f32_32x32x32_bf16 v[100:115], v[152:155], v[24:31], v164\n"
"v_exp_f32 v34, v34\n"
"v_exp_f32 v35, v35\n"
"v_exp_f32 v36, v36\n"
"v_mfma_f32_32x32x16_bf16 v[16:31], v[144:147], v[132:135], v[48:63]\n"
"v_exp_f32 v37, v37\n"
"v_exp_f32 v38, v38\n"
"v_exp_f32 v39, v39\n"
"v_exp_f32 v40, v40\n"
"v_exp_f32 v41, v41\n"
"v_exp_f32 v42, v42\n"
"v_smfmac_f32_32x32x32_bf16 v[116:131], v[148:151], v[32:39], v164\n"
"v_exp_f32 v43, v43\n"
"v_exp_f32 v44, v44\n"
"v_exp_f32 v45, v45\n"
"v_exp_f32 v46, v46\n"
"v_exp_f32 v47, v47\n"
"v_exp_f32 v16, v16\n"
"v_exp_f32 v17, v17\n"
"v_smfmac_f32_32x32x32_bf16 v[116:131], v[152:155], v[40:47], v164\n"
"s_waitcnt lgkmcnt(0)\n"
"v_exp_f32 v18, v18\n"
"v_exp_f32 v19, v19\n"
"v_exp_f32 v20, v20\n"
"v_mfma_f32_32x32x16_bf16 v[32:47], v[144:147], v[136:139], v[64:79]\n"
"v_exp_f32 v21, v21\n"
"v_exp_f32 v22, v22\n"
"v_exp_f32 v23, v23\n"
"v_exp_f32 v24, v24\n"
"v_exp_f32 v25, v25\n"
"v_exp_f32 v26, v26\n"
"v_smfmac_f32_32x32x32_bf16 v[100:115], v[156:159], v[16:23], v164\n"
"v_exp_f32 v27, v27\n"
"v_exp_f32 v28, v28\n"
"v_exp_f32 v29, v29\n"
"v_exp_f32 v30, v30\n"
"v_exp_f32 v31, v31\n"
"v_exp_f32 v32, v32\n"
"v_exp_f32 v33, v33\n"
"v_smfmac_f32_32x32x32_bf16 v[100:115], v[160:163], v[24:31], v164\n"
"s_cmp_ge_u32 s68, 4\n"
"s_cbranch_scc1 Lcls_115_%=\n"
"s_mov_b32 s82, 7868416\n"
"buffer_load_dwordx2 v[186:187], v246, s[40:43], s82 offen sc1\n"
"Lcls_115_%=:\n"
"v_exp_f32 v34, v34\n"
"v_exp_f32 v35, v35\n"
"v_exp_f32 v36, v36\n"
"v_exp_f32 v37, v37\n"
"v_exp_f32 v38, v38\n"
"v_exp_f32 v39, v39\n"
"v_exp_f32 v40, v40\n"
"v_exp_f32 v41, v41\n"
"v_exp_f32 v42, v42\n"
"v_smfmac_f32_32x32x32_bf16 v[116:131], v[156:159], v[32:39], v164\n"
"v_exp_f32 v43, v43\n"
"v_exp_f32 v44, v44\n"
"v_exp_f32 v45, v45\n"
"v_exp_f32 v46, v46\n"
"v_exp_f32 v47, v47\n"
"s_nop 1\n"
"v_smfmac_f32_32x32x32_bf16 v[116:131], v[160:163], v[40:47], v164\n"
"s_cmp_ge_u32 s68, 4\n"
"s_cbranch_scc1 Lbfin_116_%=\n"
"ds_write_b128 v242, v[100:103] offset:0\n"
"ds_write_b128 v242, v[104:107] offset:1024\n"
"s_nop 7\n"
"ds_write_b128 v242, v[116:119] offset:2048\n"
"ds_write_b128 v242, v[120:123] offset:3072\n"
"ds_write2st64_b32 v229, v108, v124 offset0:0 offset1:1\n"
"v_lshrrev_b32 v6, 3, v1\n"
"v_and_b32 v5, 7, v1\n"
"v_and_b32 v7, 31, v1\n"
"v_lshrrev_b32 v8, 5, v1\n"
"v_lshrrev_b32 v9, 4, v1\n"
"v_bfe_u32 v10, v1, 2, 2\n"
"v_and_b32 v11, 3, v1\n"
"s_mov_b32 s44, s22\n"
"s_and_b32 s45, s23, 0xffff\n"
"s_mov_b32 s46, 1409024\n"
"s_mov_b32 s47, 0x20000\n"
"s_mov_b32 s48, s16\n"
"s_and_b32 s49, s17, 0xffff\n"
"s_mov_b32 s50, 524288\n"
"s_mov_b32 s51, 0x20000\n"
"s_mov_b32 s52, s26\n"
"s_and_b32 s53, s27, 0xffff\n"
"s_mov_b32 s54, 2097152\n"
"s_mov_b32 s55, 0x20000\n"
"v_and_b32 v42, 1, v9\n"
"v_lshlrev_b32 v40, 2, v42\n"
"v_xor_b32 v40, v40, v5\n"
"v_lshlrev_b32 v40, 4, v40\n"
"v_lshl_add_u32 v40, v6, 10, v40\n"
"s_lshl_b32 s75, s69, 7\n"
"v_add_u32 v243, s75, v40\n"
"s_mul_i32 s74, s70, 172\n"
"s_mul_i32 s76, s71, 22\n"
"s_cmp_eq_u32 s71, 3\n"
"s_subb_u32 s76, s76, 0\n"
"s_lshl_b32 s92, s76, 14\n"
"s_lshl_b32 s93, s71, 17\n"
"s_add_u32 s76, s76, s74\n"
"s_lshl_b32 s76, s76, 10\n"
"s_add_u32 s95, s76, 131072\n"
"s_lshl_b32 s76, s70, 3\n"
"s_add_u32 s76, s76, s71\n"
"s_lshl_b32 s76, s76, 13\n"
"s_add_u32 s96, s76, 5767168\n"
"v_lshl_add_u32 v40, v8, 2, v10\n"
"v_lshlrev_b32 v40, 7, v40\n"
"v_lshl_add_u32 v40, v11, 3, v40\n"
"v_add_u32 v40, s80, v40\n"
"v_lshrrev_b32 v41, 1, v10\n"
"v_add_u32 v43, 0, v42\n"
"v_xor_b32 v43, s71, v43\n"
"v_lshrrev_b32 v44, 1, v43\n"
"v_xor_b32 v44, v44, v41\n"
"v_lshl_add_u32 v44, v44, 6, v40\n"
"v_and_b32 v43, 1, v43\n"
"v_lshl_add_u32 v244, v43, 5, v44\n"
"v_add_u32 v43, 2, v42\n"
"v_xor_b32 v43, s71, v43\n"
"v_lshrrev_b32 v44, 1, v43\n"
"v_xor_b32 v44, v44, v41\n"
"v_lshl_add_u32 v44, v44, 6, v40\n"
"v_and_b32 v43, 1, v43\n"
"v_lshl_add_u32 v245, v43, 5, v44\n"
"s_lshl_b32 s76, s71, 5\n"
"s_add_u32 s97, s75, s76\n"
"s_lshl_b32 s76, s70, 16\n"
"s_add_u32 s87, s97, s76\n"
"v_lshlrev_b32 v248, 3, v8\n"
"v_lshl_add_u32 v247, v7, 10, v248\n"
"v_lshl_add_u32 v249, v8, 3, v247\n"
"s_xor_b32 s76, s71, 0\n"
"s_mul_i32 s76, s76, 14336\n"
"s_add_u32 s76, s76, 0\n"
"v_add_u32 v250, s76, v2\n"
"s_xor_b32 s76, s71, 1\n"
"s_mul_i32 s76, s76, 14336\n"
"s_add_u32 s76, s76, 2048\n"
"v_add_u32 v251, s76, v2\n"
"s_xor_b32 s76, s71, 2\n"
"s_mul_i32 s76, s76, 14336\n"
"s_add_u32 s76, s76, 4096\n"
"v_add_u32 v252, s76, v2\n"
"s_xor_b32 s76, s71, 3\n"
"s_mul_i32 s76, s76, 14336\n"
"s_add_u32 s76, s76, 6144\n"
"v_add_u32 v253, s76, v2\n"
"Lhasbeta_117_%=:\n"
"s_mov_b32 s75, 524288\n"
"Lhspin_119_%=:\n"
"s_waitcnt vmcnt(0)\n"
"v_cmp_ne_u64 vcc, s[72:73], v[186:187]\n"
"s_nop 0\n"
"s_cbranch_vccz Lhok_118_%=\n"
"s_sleep 2\n"
"buffer_load_dwordx2 v[186:187], v246, s[40:43], s82 offen sc1\n"
"s_sub_u32 s75, s75, 1\n"
"s_cmp_lg_u32 s75, 0\n"
"s_cbranch_scc1 Lhspin_119_%=\n"
"s_waitcnt vmcnt(0)\n"
"Lhok_118_%=:\n"
"s_add_u32 s84, s95, 88064\n"
"buffer_load_dwordx4 v[96:99], v2, s[40:43], s95 offen offset:0 sc1\n"
"buffer_load_dwordx4 v[100:103], v2, s[40:43], s84 offen offset:0 sc1\n"
"buffer_load_dwordx4 v[104:107], v2, s[40:43], s95 offen offset:1024 sc1\n"
"buffer_load_dwordx4 v[108:111], v2, s[40:43], s84 offen offset:1024 sc1\n"
"buffer_load_dwordx4 v[112:115], v2, s[40:43], s95 offen offset:2048 sc1\n"
"buffer_load_dwordx4 v[116:119], v2, s[40:43], s84 offen offset:2048 sc1\n"
"buffer_load_dwordx4 v[120:123], v2, s[40:43], s95 offen offset:3072 sc1\n"
"buffer_load_dwordx4 v[124:127], v2, s[40:43], s84 offen offset:3072 sc1\n"
"s_add_u32 s95, s95, 4096\n"
"s_add_u32 s84, s84, 4096\n"
"buffer_load_dwordx4 v[128:131], v2, s[40:43], s95 offen offset:0 sc1\n"
"buffer_load_dwordx4 v[132:135], v2, s[40:43], s84 offen offset:0 sc1\n"
"buffer_load_dwordx4 v[136:139], v2, s[40:43], s95 offen offset:1024 sc1\n"
"buffer_load_dwordx4 v[140:143], v2, s[40:43], s84 offen offset:1024 sc1\n"
"buffer_load_dwordx4 v[144:147], v2, s[40:43], s95 offen offset:2048 sc1\n"
"buffer_load_dwordx4 v[148:151], v2, s[40:43], s84 offen offset:2048 sc1\n"
"s_setprio 0\n"
"s_waitcnt lgkmcnt(0)\n"
"s_barrier\n"
"s_add_u32 m0, s80, 0\n"
"s_add_u32 s74, s92, 0\n"
"buffer_load_dwordx4 v243, s[44:47], s74 offen lds\n"
"s_add_u32 m0, s80, 1024\n"
"s_add_u32 s74, s92, 8192\n"
"buffer_load_dwordx4 v243, s[44:47], s74 offen lds\n"
"s_add_u32 m0, s80, 2048\n"
"s_add_u32 s74, s92, 16384\n"
"buffer_load_dwordx4 v243, s[44:47], s74 offen lds\n"
"s_add_u32 m0, s80, 3072\n"
"s_add_u32 s74, s92, 24576\n"
"buffer_load_dwordx4 v243, s[44:47], s74 offen lds\n"
"s_add_u32 m0, s80, 4096\n"
"s_add_u32 s74, s92, 32768\n"
"buffer_load_dwordx4 v243, s[44:47], s74 offen lds\n"
"s_add_u32 m0, s80, 5120\n"
"s_add_u32 s74, s92, 40960\n"
"buffer_load_dwordx4 v243, s[44:47], s74 offen lds\n"
"s_add_u32 m0, s80, 6144\n"
"s_add_u32 s74, s92, 49152\n"
"buffer_load_dwordx4 v243, s[44:47], s74 offen lds\n"
"s_add_u32 m0, s80, 7168\n"
"s_add_u32 s74, s92, 57344\n"
"buffer_load_dwordx4 v243, s[44:47], s74 offen lds\n"
"s_add_u32 m0, s80, 8192\n"
"s_add_u32 s74, s92, 65536\n"
"buffer_load_dwordx4 v243, s[44:47], s74 offen lds\n"
"s_add_u32 m0, s80, 9216\n"
"s_add_u32 s74, s92, 73728\n"
"buffer_load_dwordx4 v243, s[44:47], s74 offen lds\n"
"s_add_u32 m0, s80, 10240\n"
"s_add_u32 s74, s92, 81920\n"
"buffer_load_dwordx4 v243, s[44:47], s74 offen lds\n"
"s_add_u32 m0, s80, 11264\n"
"s_add_u32 s74, s92, 90112\n"
"buffer_load_dwordx4 v243, s[44:47], s74 offen lds\n"
"s_add_u32 m0, s80, 12288\n"
"s_add_u32 s74, s92, 98304\n"
"buffer_load_dwordx4 v243, s[44:47], s74 offen lds\n"
"s_add_u32 m0, s80, 13312\n"
"s_add_u32 s74, s92, 106496\n"
"buffer_load_dwordx4 v243, s[44:47], s74 offen lds\n"
"s_waitcnt vmcnt(12)\n"
"ds_read_b64_tr_b16 v[80:81], v244 offset:0\n"
"ds_read_b64_tr_b16 v[82:83], v244 offset:1024\n"
"ds_read_b64_tr_b16 v[84:85], v245 offset:0\n"
"ds_read_b64_tr_b16 v[86:87], v245 offset:1024\n"
"s_waitcnt vmcnt(10)\n"
"s_waitcnt lgkmcnt(0)\n"
"v_mfma_f32_32x32x16_bf16 v[16:31], v[80:83], v[96:99], 0\n"
"ds_read_b64_tr_b16 v[88:89], v244 offset:2048\n"
"ds_read_b64_tr_b16 v[90:91], v244 offset:3072\n"
"ds_read_b64_tr_b16 v[92:93], v245 offset:2048\n"
"ds_read_b64_tr_b16 v[94:95], v245 offset:3072\n"
"v_mfma_f32_32x32x16_bf16 v[32:47], v[84:87], v[96:99], 0\n"
"s_add_u32 m0, s80, 14336\n"
"s_add_u32 s74, s92, 114688\n"
"buffer_load_dwordx4 v243, s[44:47], s74 offen lds\n"
"s_add_u32 m0, s80, 15360\n"
"s_add_u32 s74, s92, 122880\n"
"buffer_load_dwordx4 v243, s[44:47], s74 offen lds\n"
"v_mfma_f32_32x32x16_bf16 v[48:63], v[80:83], v[100:103], 0\n"
"v_mfma_f32_32x32x16_bf16 v[64:79], v[84:87], v[100:103], 0\n"
"buffer_load_dwordx4 v[152:155], v2, s[40:43], s95 offen offset:3072 sc1\n"
"buffer_load_dwordx4 v[156:159], v2, s[40:43], s84 offen offset:3072 sc1\n"
"s_waitcnt vmcnt(12)\n"
"s_waitcnt lgkmcnt(0)\n"
"v_mfma_f32_32x32x16_bf16 v[16:31], v[88:91], v[104:107], v[16:31]\n"
"ds_read_b64_tr_b16 v[80:81], v244 offset:4096\n"
"ds_read_b64_tr_b16 v[82:83], v244 offset:5120\n"
"ds_read_b64_tr_b16 v[84:85], v245 offset:4096\n"
"ds_read_b64_tr_b16 v[86:87], v245 offset:5120\n"
"v_mfma_f32_32x32x16_bf16 v[32:47], v[92:95], v[104:107], v[32:47]\n"
"s_add_u32 m0, s80, 0\n"
"s_add_u32 s74, s92, 131072\n"
"buffer_load_dwordx4 v243, s[44:47], s74 offen lds\n"
"s_add_u32 m0, s80, 1024\n"
"s_add_u32 s74, s92, 139264\n"
"buffer_load_dwordx4 v243, s[44:47], s74 offen lds\n"
"v_mfma_f32_32x32x16_bf16 v[48:63], v[88:91], v[108:111], v[48:63]\n"
"v_mfma_f32_32x32x16_bf16 v[64:79], v[92:95], v[108:111], v[64:79]\n"
"s_add_u32 s95, s95, 4096\n"
"s_add_u32 s84, s84, 4096\n"
"buffer_load_dwordx4 v[96:99], v2, s[40:43], s95 offen offset:0 sc1\n"
"buffer_load_dwordx4 v[100:103], v2, s[40:43], s84 offen offset:0 sc1\n"
"s_waitcnt vmcnt(14)\n"
"s_waitcnt lgkmcnt(0)\n"
"v_mfma_f32_32x32x16_bf16 v[16:31], v[80:83], v[112:115], v[16:31]\n"
"ds_read_b64_tr_b16 v[88:89], v244 offset:6144\n"
"ds_read_b64_tr_b16 v[90:91], v244 offset:7168\n"
"ds_read_b64_tr_b16 v[92:93], v245 offset:6144\n"
"ds_read_b64_tr_b16 v[94:95], v245 offset:7168\n"
"v_mfma_f32_32x32x16_bf16 v[32:47], v[84:87], v[112:115], v[32:47]\n"
"s_add_u32 m0, s80, 2048\n"
"s_add_u32 s74, s92, 147456\n"
"buffer_load_dwordx4 v243, s[44:47], s74 offen lds\n"
"s_add_u32 m0, s80, 3072\n"
"s_add_u32 s74, s92, 155648\n"
"buffer_load_dwordx4 v243, s[44:47], s74 offen lds\n"
"v_mfma_f32_32x32x16_bf16 v[48:63], v[80:83], v[116:119], v[48:63]\n"
"v_mfma_f32_32x32x16_bf16 v[64:79], v[84:87], v[116:119], v[64:79]\n"
"buffer_load_dwordx4 v[104:107], v2, s[40:43], s95 offen offset:1024 sc1\n"
"buffer_load_dwordx4 v[108:111], v2, s[40:43], s84 offen offset:1024 sc1\n"
"s_waitcnt vmcnt(16)\n"
"s_waitcnt lgkmcnt(0)\n"
"v_mfma_f32_32x32x16_bf16 v[16:31], v[88:91], v[120:123], v[16:31]\n"
"ds_read_b64_tr_b16 v[80:81], v244 offset:8192\n"
"ds_read_b64_tr_b16 v[82:83], v244 offset:9216\n"
"ds_read_b64_tr_b16 v[84:85], v245 offset:8192\n"
"ds_read_b64_tr_b16 v[86:87], v245 offset:9216\n"
"v_mfma_f32_32x32x16_bf16 v[32:47], v[92:95], v[120:123], v[32:47]\n"
"s_add_u32 m0, s80, 4096\n"
"s_add_u32 s74, s92, 163840\n"
"buffer_load_dwordx4 v243, s[44:47], s74 offen lds\n"
"s_add_u32 m0, s80, 5120\n"
"s_add_u32 s74, s92, 172032\n"
"buffer_load_dwordx4 v243, s[44:47], s74 offen lds\n"
"v_mfma_f32_32x32x16_bf16 v[48:63], v[88:91], v[124:127], v[48:63]\n"
"v_mfma_f32_32x32x16_bf16 v[64:79], v[92:95], v[124:127], v[64:79]\n"
"buffer_load_dwordx4 v[112:115], v2, s[40:43], s95 offen offset:2048 sc1\n"
"buffer_load_dwordx4 v[116:119], v2, s[40:43], s84 offen offset:2048 sc1\n"
"s_waitcnt vmcnt(18)\n"
"s_waitcnt lgkmcnt(0)\n"
"v_mfma_f32_32x32x16_bf16 v[16:31], v[80:83], v[128:131], v[16:31]\n"
"ds_read_b64_tr_b16 v[88:89], v244 offset:10240\n"
"ds_read_b64_tr_b16 v[90:91], v244 offset:11264\n"
"ds_read_b64_tr_b16 v[92:93], v245 offset:10240\n"
"ds_read_b64_tr_b16 v[94:95], v245 offset:11264\n"
"v_mfma_f32_32x32x16_bf16 v[32:47], v[84:87], v[128:131], v[32:47]\n"
"s_add_u32 m0, s80, 6144\n"
"s_add_u32 s74, s92, 180224\n"
"buffer_load_dwordx4 v243, s[44:47], s74 offen lds\n"
"s_add_u32 m0, s80, 7168\n"
"s_add_u32 s74, s92, 188416\n"
"buffer_load_dwordx4 v243, s[44:47], s74 offen lds\n"
"v_mfma_f32_32x32x16_bf16 v[48:63], v[80:83], v[132:135], v[48:63]\n"
"v_mfma_f32_32x32x16_bf16 v[64:79], v[84:87], v[132:135], v[64:79]\n"
"buffer_load_dwordx4 v[120:123], v2, s[40:43], s95 offen offset:3072 sc1\n"
"buffer_load_dwordx4 v[124:127], v2, s[40:43], s84 offen offset:3072 sc1\n"
"s_waitcnt vmcnt(20)\n"
"s_waitcnt lgkmcnt(0)\n"
"v_mfma_f32_32x32x16_bf16 v[16:31], v[88:91], v[136:139], v[16:31]\n"
"ds_read_b64_tr_b16 v[80:81], v244 offset:12288\n"
"ds_read_b64_tr_b16 v[82:83], v244 offset:13312\n"
"ds_read_b64_tr_b16 v[84:85], v245 offset:12288\n"
"ds_read_b64_tr_b16 v[86:87], v245 offset:13312\n"
"v_mfma_f32_32x32x16_bf16 v[32:47], v[92:95], v[136:139], v[32:47]\n"
"s_add_u32 m0, s80, 8192\n"
"s_add_u32 s74, s92, 196608\n"
"buffer_load_dwordx4 v243, s[44:47], s74 offen lds\n"
"s_add_u32 m0, s80, 9216\n"
"s_add_u32 s74, s92, 204800\n"
"buffer_load_dwordx4 v243, s[44:47], s74 offen lds\n"
"v_mfma_f32_32x32x16_bf16 v[48:63], v[88:91], v[140:143], v[48:63]\n"
"v_mfma_f32_32x32x16_bf16 v[64:79], v[92:95], v[140:143], v[64:79]\n"
"s_add_u32 s95, s95, 4096\n"
"s_add_u32 s84, s84, 4096\n"
"buffer_load_dwordx4 v[128:131], v2, s[40:43], s95 offen offset:0 sc1\n"
"buffer_load_dwordx4 v[132:135], v2, s[40:43], s84 offen offset:0 sc1\n"
"s_waitcnt vmcnt(22)\n"
"s_waitcnt lgkmcnt(0)\n"
"v_mfma_f32_32x32x16_bf16 v[16:31], v[80:83], v[144:147], v[16:31]\n"
"ds_read_b64_tr_b16 v[88:89], v244 offset:14336\n"
"ds_read_b64_tr_b16 v[90:91], v244 offset:15360\n"
"ds_read_b64_tr_b16 v[92:93], v245 offset:14336\n"
"ds_read_b64_tr_b16 v[94:95], v245 offset:15360\n"
"v_mfma_f32_32x32x16_bf16 v[32:47], v[84:87], v[144:147], v[32:47]\n"
"s_add_u32 m0, s80, 10240\n"
"s_add_u32 s74, s92, 212992\n"
"buffer_load_dwordx4 v243, s[44:47], s74 offen lds\n"
"s_add_u32 m0, s80, 11264\n"
"s_add_u32 s74, s92, 221184\n"
"buffer_load_dwordx4 v243, s[44:47], s74 offen lds\n"
"v_mfma_f32_32x32x16_bf16 v[48:63], v[80:83], v[148:151], v[48:63]\n"
"v_mfma_f32_32x32x16_bf16 v[64:79], v[84:87], v[148:151], v[64:79]\n"
"buffer_load_dwordx4 v[136:139], v2, s[40:43], s95 offen offset:1024 sc1\n"
"buffer_load_dwordx4 v[140:143], v2, s[40:43], s84 offen offset:1024 sc1\n"
"s_waitcnt vmcnt(22)\n"
"s_waitcnt lgkmcnt(0)\n"
"v_mfma_f32_32x32x16_bf16 v[16:31], v[88:91], v[152:155], v[16:31]\n"
"ds_read_b64_tr_b16 v[80:81], v244 offset:0\n"
"ds_read_b64_tr_b16 v[82:83], v244 offset:1024\n"
"ds_read_b64_tr_b16 v[84:85], v245 offset:0\n"
"ds_read_b64_tr_b16 v[86:87], v245 offset:1024\n"
"v_mfma_f32_32x32x16_bf16 v[32:47], v[92:95], v[152:155], v[32:47]\n"
"s_add_u32 m0, s80, 12288\n"
"s_add_u32 s74, s92, 229376\n"
"buffer_load_dwordx4 v243, s[44:47], s74 offen lds\n"
"s_add_u32 m0, s80, 13312\n"
"s_add_u32 s74, s92, 237568\n"
"buffer_load_dwordx4 v243, s[44:47], s74 offen lds\n"
"v_mfma_f32_32x32x16_bf16 v[48:63], v[88:91], v[156:159], v[48:63]\n"
"v_mfma_f32_32x32x16_bf16 v[64:79], v[92:95], v[156:159], v[64:79]\n"
"buffer_load_dwordx4 v[144:147], v2, s[40:43], s95 offen offset:2048 sc1\n"
"buffer_load_dwordx4 v[148:151], v2, s[40:43], s84 offen offset:2048 sc1\n"
"s_waitcnt vmcnt(22)\n"
"s_waitcnt lgkmcnt(0)\n"
"v_mfma_f32_32x32x16_bf16 v[16:31], v[80:83], v[96:99], v[16:31]\n"
"ds_read_b64_tr_b16 v[88:89], v244 offset:2048\n"
"ds_read_b64_tr_b16 v[90:91], v244 offset:3072\n"
"ds_read_b64_tr_b16 v[92:93], v245 offset:2048\n"
"ds_read_b64_tr_b16 v[94:95], v245 offset:3072\n"
"v_mfma_f32_32x32x16_bf16 v[32:47], v[84:87], v[96:99], v[32:47]\n"
"s_add_u32 m0, s80, 14336\n"
"s_add_u32 s74, s93, 0\n"
"buffer_load_dwordx4 v243, s[48:51], s74 offen lds\n"
"s_add_u32 m0, s80, 15360\n"
"s_add_u32 s74, s93, 8192\n"
"buffer_load_dwordx4 v243, s[48:51], s74 offen lds\n"
"v_mfma_f32_32x32x16_bf16 v[48:63], v[80:83], v[100:103], v[48:63]\n"
"v_mfma_f32_32x32x16_bf16 v[64:79], v[84:87], v[100:103], v[64:79]\n"
"buffer_load_dwordx4 v[152:155], v2, s[40:43], s95 offen offset:3072 sc1\n"
"buffer_load_dwordx4 v[156:159], v2, s[40:43], s84 offen offset:3072 sc1\n"
"s_waitcnt vmcnt(22)\n"
"s_waitcnt lgkmcnt(0)\n"
"v_mfma_f32_32x32x16_bf16 v[16:31], v[88:91], v[104:107], v[16:31]\n"
"ds_read_b64_tr_b16 v[80:81], v244 offset:4096\n"
"ds_read_b64_tr_b16 v[82:83], v244 offset:5120\n"
"ds_read_b64_tr_b16 v[84:85], v245 offset:4096\n"
"ds_read_b64_tr_b16 v[86:87], v245 offset:5120\n"
"v_mfma_f32_32x32x16_bf16 v[32:47], v[92:95], v[104:107], v[32:47]\n"
"s_add_u32 m0, s80, 0\n"
"s_add_u32 s74, s93, 16384\n"
"buffer_load_dwordx4 v243, s[48:51], s74 offen lds\n"
"s_add_u32 m0, s80, 1024\n"
"s_add_u32 s74, s93, 24576\n"
"buffer_load_dwordx4 v243, s[48:51], s74 offen lds\n"
"v_mfma_f32_32x32x16_bf16 v[48:63], v[88:91], v[108:111], v[48:63]\n"
"v_mfma_f32_32x32x16_bf16 v[64:79], v[92:95], v[108:111], v[64:79]\n"
"s_add_u32 s95, s95, 4096\n"
"s_add_u32 s84, s84, 4096\n"
"buffer_load_dwordx4 v[96:99], v2, s[40:43], s95 offen offset:0 sc1\n"
"buffer_load_dwordx4 v[100:103], v2, s[40:43], s84 offen offset:0 sc1\n"
"s_waitcnt vmcnt(22)\n"
"s_waitcnt lgkmcnt(0)\n"
"v_mfma_f32_32x32x16_bf16 v[16:31], v[80:83], v[112:115], v[16:31]\n"
"ds_read_b64_tr_b16 v[88:89], v244 offset:6144\n"
"ds_read_b64_tr_b16 v[90:91], v244 offset:7168\n"
"ds_read_b64_tr_b16 v[92:93], v245 offset:6144\n"
"ds_read_b64_tr_b16 v[94:95], v245 offset:7168\n"
"v_mfma_f32_32x32x16_bf16 v[32:47], v[84:87], v[112:115], v[32:47]\n"
"s_add_u32 m0, s80, 2048\n"
"s_add_u32 s74, s93, 32768\n"
"buffer_load_dwordx4 v243, s[48:51], s74 offen lds\n"
"s_add_u32 m0, s80, 3072\n"
"s_add_u32 s74, s93, 40960\n"
"buffer_load_dwordx4 v243, s[48:51], s74 offen lds\n"
"v_mfma_f32_32x32x16_bf16 v[48:63], v[80:83], v[116:119], v[48:63]\n"
"v_mfma_f32_32x32x16_bf16 v[64:79], v[84:87], v[116:119], v[64:79]\n"
"buffer_load_dwordx4 v[104:107], v2, s[40:43], s95 offen offset:1024 sc1\n"
"buffer_load_dwordx4 v[108:111], v2, s[40:43], s84 offen offset:1024 sc1\n"
"s_waitcnt vmcnt(22)\n"
"s_waitcnt lgkmcnt(0)\n"
"v_mfma_f32_32x32x16_bf16 v[16:31], v[88:91], v[120:123], v[16:31]\n"
"ds_read_b64_tr_b16 v[80:81], v244 offset:8192\n"
"ds_read_b64_tr_b16 v[82:83], v244 offset:9216\n"
"ds_read_b64_tr_b16 v[84:85], v245 offset:8192\n"
"ds_read_b64_tr_b16 v[86:87], v245 offset:9216\n"
"v_mfma_f32_32x32x16_bf16 v[32:47], v[92:95], v[120:123], v[32:47]\n"
"s_add_u32 m0, s80, 4096\n"
"s_add_u32 s74, s93, 49152\n"
"buffer_load_dwordx4 v243, s[48:51], s74 offen lds\n"
"s_add_u32 m0, s80, 5120\n"
"s_add_u32 s74, s93, 57344\n"
"buffer_load_dwordx4 v243, s[48:51], s74 offen lds\n"
"v_mfma_f32_32x32x16_bf16 v[48:63], v[88:91], v[124:127], v[48:63]\n"
"v_mfma_f32_32x32x16_bf16 v[64:79], v[92:95], v[124:127], v[64:79]\n"
"buffer_load_dwordx4 v[112:115], v2, s[40:43], s95 offen offset:2048 sc1\n"
"buffer_load_dwordx4 v[116:119], v2, s[40:43], s84 offen offset:2048 sc1\n"
"s_waitcnt vmcnt(22)\n"
"s_waitcnt lgkmcnt(0)\n"
"v_mfma_f32_32x32x16_bf16 v[16:31], v[80:83], v[128:131], v[16:31]\n"
"ds_read_b64_tr_b16 v[88:89], v244 offset:10240\n"
"ds_read_b64_tr_b16 v[90:91], v244 offset:11264\n"
"ds_read_b64_tr_b16 v[92:93], v245 offset:10240\n"
"ds_read_b64_tr_b16 v[94:95], v245 offset:11264\n"
"v_mfma_f32_32x32x16_bf16 v[32:47], v[84:87], v[128:131], v[32:47]\n"
"s_add_u32 m0, s80, 6144\n"
"s_add_u32 s74, s93, 65536\n"
"buffer_load_dwordx4 v243, s[48:51], s74 offen lds\n"
"s_add_u32 m0, s80, 7168\n"
"s_add_u32 s74, s93, 73728\n"
"buffer_load_dwordx4 v243, s[48:51], s74 offen lds\n"
"v_mfma_f32_32x32x16_bf16 v[48:63], v[80:83], v[132:135], v[48:63]\n"
"v_mfma_f32_32x32x16_bf16 v[64:79], v[84:87], v[132:135], v[64:79]\n"
"buffer_load_dwordx4 v[120:123], v2, s[40:43], s95 offen offset:3072 sc1\n"
"buffer_load_dwordx4 v[124:127], v2, s[40:43], s84 offen offset:3072 sc1\n"
"s_waitcnt vmcnt(22)\n"
"s_waitcnt lgkmcnt(0)\n"
"v_mfma_f32_32x32x16_bf16 v[16:31], v[88:91], v[136:139], v[16:31]\n"
"ds_read_b64_tr_b16 v[80:81], v244 offset:12288\n"
"ds_read_b64_tr_b16 v[82:83], v244 offset:13312\n"
"ds_read_b64_tr_b16 v[84:85], v245 offset:12288\n"
"ds_read_b64_tr_b16 v[86:87], v245 offset:13312\n"
"v_mfma_f32_32x32x16_bf16 v[32:47], v[92:95], v[136:139], v[32:47]\n"
"s_add_u32 m0, s80, 8192\n"
"s_add_u32 s74, s93, 81920\n"
"buffer_load_dwordx4 v243, s[48:51], s74 offen lds\n"
"s_add_u32 m0, s80, 9216\n"
"s_add_u32 s74, s93, 90112\n"
"buffer_load_dwordx4 v243, s[48:51], s74 offen lds\n"
"v_mfma_f32_32x32x16_bf16 v[48:63], v[88:91], v[140:143], v[48:63]\n"
"v_mfma_f32_32x32x16_bf16 v[64:79], v[92:95], v[140:143], v[64:79]\n"
"s_add_u32 s95, s95, 4096\n"
"s_add_u32 s84, s84, 4096\n"
"buffer_load_dwordx4 v[128:131], v2, s[40:43], s95 offen offset:0 sc1\n"
"buffer_load_dwordx4 v[132:135], v2, s[40:43], s84 offen offset:0 sc1\n"
"s_waitcnt vmcnt(22)\n"
"s_waitcnt lgkmcnt(0)\n"
"v_mfma_f32_32x32x16_bf16 v[16:31], v[80:83], v[144:147], v[16:31]\n"
"ds_read_b64_tr_b16 v[160:161], v244 offset:14336\n"
"ds_read_b64_tr_b16 v[162:163], v244 offset:15360\n"
"ds_read_b64_tr_b16 v[164:165], v245 offset:14336\n"
"ds_read_b64_tr_b16 v[166:167], v245 offset:15360\n"
"v_mfma_f32_32x32x16_bf16 v[32:47], v[84:87], v[144:147], v[32:47]\n"
"s_add_u32 m0, s80, 10240\n"
"s_add_u32 s74, s93, 98304\n"
"buffer_load_dwordx4 v243, s[48:51], s74 offen lds\n"
"s_add_u32 m0, s80, 11264\n"
"s_add_u32 s74, s93, 106496\n"
"buffer_load_dwordx4 v243, s[48:51], s74 offen lds\n"
"v_mfma_f32_32x32x16_bf16 v[48:63], v[80:83], v[148:151], v[48:63]\n"
"v_mfma_f32_32x32x16_bf16 v[64:79], v[84:87], v[148:151], v[64:79]\n"
"s_cmp_ge_u32 s71, 2\n"
"s_cselect_b32 s75, 0x7f000000, s95\n"
"s_cselect_b32 s76, 0x7f000000, s84\n"
"buffer_load_dwordx4 v[136:139], v2, s[40:43], s75 offen offset:1024 sc1\n"
"buffer_load_dwordx4 v[140:143], v2, s[40:43], s76 offen offset:1024 sc1\n"
"s_waitcnt vmcnt(22)\n"
"s_waitcnt lgkmcnt(0)\n"
"ds_read_b64_tr_b16 v[168:169], v244 offset:0\n"
"ds_read_b64_tr_b16 v[170:171], v244 offset:1024\n"
"ds_read_b64_tr_b16 v[172:173], v245 offset:0\n"
"ds_read_b64_tr_b16 v[174:175], v245 offset:1024\n"
"s_add_u32 m0, s80, 12288\n"
"s_add_u32 s74, s93, 114688\n"
"buffer_load_dwordx4 v243, s[48:51], s74 offen lds\n"
"s_add_u32 m0, s80, 13312\n"
"s_add_u32 s74, s93, 122880\n"
"buffer_load_dwordx4 v243, s[48:51], s74 offen lds\n"
"s_waitcnt vmcnt(20)\n"
"s_waitcnt lgkmcnt(0)\n"
"ds_read_b64_tr_b16 v[176:177], v244 offset:2048\n"
"ds_read_b64_tr_b16 v[178:179], v244 offset:3072\n"
"ds_read_b64_tr_b16 v[180:181], v245 offset:2048\n"
"ds_read_b64_tr_b16 v[182:183], v245 offset:3072\n"
"s_add_u32 m0, s80, 14336\n"
"s_add_u32 s74, s92, 245760\n"
"buffer_load_dwordx4 v243, s[44:47], s74 offen lds\n"
"s_add_u32 m0, s80, 15360\n"
"s_add_u32 s74, s92, 253952\n"
"buffer_load_dwordx4 v243, s[44:47], s74 offen lds\n"
"s_waitcnt vmcnt(18)\n"
"s_waitcnt lgkmcnt(0)\n"
"ds_read_b64_tr_b16 v[184:185], v244 offset:4096\n"
"ds_read_b64_tr_b16 v[186:187], v244 offset:5120\n"
"ds_read_b64_tr_b16 v[188:189], v245 offset:4096\n"
"ds_read_b64_tr_b16 v[190:191], v245 offset:5120\n"
"s_add_u32 m0, s80, 0\n"
"s_add_u32 s74, s92, 262144\n"
"buffer_load_dwordx4 v243, s[44:47], s74 offen lds\n"
"s_add_u32 m0, s80, 1024\n"
"s_add_u32 s74, s92, 270336\n"
"buffer_load_dwordx4 v243, s[44:47], s74 offen lds\n"
"s_waitcnt vmcnt(16)\n"
"s_waitcnt lgkmcnt(0)\n"
"ds_read_b64_tr_b16 v[192:193], v244 offset:6144\n"
"ds_read_b64_tr_b16 v[194:195], v244 offset:7168\n"
"ds_read_b64_tr_b16 v[196:197], v245 offset:6144\n"
"ds_read_b64_tr_b16 v[198:199], v245 offset:7168\n"
"s_add_u32 m0, s80, 2048\n"
"s_add_u32 s74, s92, 278528\n"
"buffer_load_dwordx4 v243, s[44:47], s74 offen lds\n"
"s_add_u32 m0, s80, 3072\n"
"s_add_u32 s74, s92, 286720\n"
"buffer_load_dwordx4 v243, s[44:47], s74 offen lds\n"
"s_add_u32 s87, s87, 32768\n"
"buffer_load_dwordx2 v[224:225], v247, s[32:35], s87 offen\n"
"buffer_load_dwordx2 v[226:227], v247, s[32:35], s87 offen offset:16\n"
"buffer_load_dwordx2 v[10:11], v248, s[60:63], s97 offen\n"
"buffer_load_dwordx2 v[12:13], v248, s[60:63], s97 offen offset:16\n"
"s_waitcnt vmcnt(18)\n"
"s_waitcnt lgkmcnt(0)\n"
"ds_read_b64_tr_b16 v[200:201], v244 offset:8192\n"
"ds_read_b64_tr_b16 v[202:203], v244 offset:9216\n"
"ds_read_b64_tr_b16 v[204:205], v245 offset:8192\n"
"ds_read_b64_tr_b16 v[206:207], v245 offset:9216\n"
"s_add_u32 m0, s80, 4096\n"
"s_add_u32 s74, s92, 294912\n"
"buffer_load_dwordx4 v243, s[44:47], s74 offen lds\n"
"s_add_u32 m0, s80, 5120\n"
"s_add_u32 s74, s92, 303104\n"
"buffer_load_dwordx4 v243, s[44:47], s74 offen lds\n"
"s_waitcnt vmcnt(16)\n"
"s_waitcnt lgkmcnt(0)\n"
"ds_read_b64_tr_b16 v[208:209], v244 offset:10240\n"
"ds_read_b64_tr_b16 v[210:211], v244 offset:11264\n"
"ds_read_b64_tr_b16 v[212:213], v245 offset:10240\n"
"ds_read_b64_tr_b16 v[214:215], v245 offset:11264\n"
"s_add_u32 m0, s80, 6144\n"
"s_add_u32 s74, s92, 311296\n"
"buffer_load_dwordx4 v243, s[44:47], s74 offen lds\n"
"s_add_u32 m0, s80, 7168\n"
"s_add_u32 s74, s92, 319488\n"
"buffer_load_dwordx4 v243, s[44:47], s74 offen lds\n"
"s_waitcnt vmcnt(14)\n"
"s_waitcnt lgkmcnt(0)\n"
"ds_read_b64_tr_b16 v[216:217], v244 offset:12288\n"
"ds_read_b64_tr_b16 v[218:219], v244 offset:13312\n"
"ds_read_b64_tr_b16 v[220:221], v245 offset:12288\n"
"ds_read_b64_tr_b16 v[222:223], v245 offset:13312\n"
"s_add_u32 m0, s80, 8192\n"
"s_add_u32 s74, s92, 327680\n"
"buffer_load_dwordx4 v243, s[44:47], s74 offen lds\n"
"s_add_u32 m0, s80, 9216\n"
"s_add_u32 s74, s92, 335872\n"
"buffer_load_dwordx4 v243, s[44:47], s74 offen lds\n"
"s_waitcnt vmcnt(14)\n"
"s_waitcnt lgkmcnt(0)\n"
"ds_read_b64_tr_b16 v[88:89], v244 offset:14336\n"
"ds_read_b64_tr_b16 v[90:91], v244 offset:15360\n"
"ds_read_b64_tr_b16 v[92:93], v245 offset:14336\n"
"ds_read_b64_tr_b16 v[94:95], v245 offset:15360\n"
"s_add_u32 m0, s80, 10240\n"
"s_add_u32 s74, s92, 344064\n"
"buffer_load_dwordx4 v243, s[44:47], s74 offen lds\n"
"s_add_u32 m0, s80, 11264\n"
"s_add_u32 s74, s92, 352256\n"
"buffer_load_dwordx4 v243, s[44:47], s74 offen lds\n"
"s_add_u32 s95, s96, 4096\n"
"s_add_u32 s84, s96, 32768\n"
"s_add_u32 s92, s96, 36864\n"
"s_waitcnt vmcnt(0) lgkmcnt(0)\n"
"s_barrier\n"
"s_setprio 3\n"
"s_waitcnt lgkmcnt(0)\n"
"v_mfma_f32_32x32x16_bf16 v[16:31], v[88:91], v[152:155], v[16:31]\n"
"ds_read_b64_tr_b16 v[80:81], v244 offset:0\n"
"ds_read_b64_tr_b16 v[82:83], v244 offset:1024\n"
"ds_read_b64_tr_b16 v[84:85], v245 offset:0\n"
"ds_read_b64_tr_b16 v[86:87], v245 offset:1024\n"
"v_mfma_f32_32x32x16_bf16 v[32:47], v[92:95], v[152:155], v[32:47]\n"
"v_mfma_f32_32x32x16_bf16 v[48:63], v[88:91], v[156:159], v[48:63]\n"
"v_mfma_f32_32x32x16_bf16 v[64:79], v[92:95], v[156:159], v[64:79]\n"
"s_waitcnt lgkmcnt(0)\n"
"v_mfma_f32_32x32x16_bf16 v[16:31], v[80:83], v[96:99], v[16:31]\n"
"ds_read_b64_tr_b16 v[88:89], v244 offset:2048\n"
"ds_read_b64_tr_b16 v[90:91], v244 offset:3072\n"
"ds_read_b64_tr_b16 v[92:93], v245 offset:2048\n"
"ds_read_b64_tr_b16 v[94:95], v245 offset:3072\n"
"v_mfma_f32_32x32x16_bf16 v[32:47], v[84:87], v[96:99], v[32:47]\n"
"v_mfma_f32_32x32x16_bf16 v[48:63], v[80:83], v[100:103], v[48:63]\n"
"v_mfma_f32_32x32x16_bf16 v[64:79], v[84:87], v[100:103], v[64:79]\n"
"s_waitcnt lgkmcnt(0)\n"
"v_mfma_f32_32x32x16_bf16 v[16:31], v[88:91], v[104:107], v[16:31]\n"
"ds_read_b64_tr_b16 v[80:81], v244 offset:4096\n"
"ds_read_b64_tr_b16 v[82:83], v244 offset:5120\n"
"ds_read_b64_tr_b16 v[84:85], v245 offset:4096\n"
"ds_read_b64_tr_b16 v[86:87], v245 offset:5120\n"
"v_mfma_f32_32x32x16_bf16 v[32:47], v[92:95], v[104:107], v[32:47]\n"
"v_mfma_f32_32x32x16_bf16 v[48:63], v[88:91], v[108:111], v[48:63]\n"
"v_mfma_f32_32x32x16_bf16 v[64:79], v[92:95], v[108:111], v[64:79]\n"
"s_waitcnt lgkmcnt(0)\n"
"v_mfma_f32_32x32x16_bf16 v[16:31], v[80:83], v[112:115], v[16:31]\n"
"ds_read_b64_tr_b16 v[88:89], v244 offset:6144\n"
"ds_read_b64_tr_b16 v[90:91], v244 offset:7168\n"
"ds_read_b64_tr_b16 v[92:93], v245 offset:6144\n"
"ds_read_b64_tr_b16 v[94:95], v245 offset:7168\n"
"v_mfma_f32_32x32x16_bf16 v[32:47], v[84:87], v[112:115], v[32:47]\n"
"v_mfma_f32_32x32x16_bf16 v[48:63], v[80:83], v[116:119], v[48:63]\n"
"v_mfma_f32_32x32x16_bf16 v[64:79], v[84:87], v[116:119], v[64:79]\n"
"s_waitcnt lgkmcnt(0)\n"
"v_mfma_f32_32x32x16_bf16 v[16:31], v[88:91], v[120:123], v[16:31]\n"
"ds_read_b64_tr_b16 v[80:81], v244 offset:8192\n"
"ds_read_b64_tr_b16 v[82:83], v244 offset:9216\n"
"ds_read_b64_tr_b16 v[84:85], v245 offset:8192\n"
"ds_read_b64_tr_b16 v[86:87], v245 offset:9216\n"
"v_mfma_f32_32x32x16_bf16 v[32:47], v[92:95], v[120:123], v[32:47]\n"
"v_mfma_f32_32x32x16_bf16 v[48:63], v[88:91], v[124:127], v[48:63]\n"
"v_mfma_f32_32x32x16_bf16 v[64:79], v[92:95], v[124:127], v[64:79]\n"
"buffer_load_dwordx4 v[144:147], v2, s[40:43], s96 offen offset:0 sc1\n"
"buffer_load_dwordx4 v[148:151], v2, s[40:43], s96 offen offset:1024 sc1\n"
"buffer_load_dwordx4 v[152:155], v2, s[40:43], s96 offen offset:2048 sc1\n"
"buffer_load_dwordx4 v[156:159], v2, s[40:43], s96 offen offset:3072 sc1\n"
"buffer_load_dwordx4 v[96:99], v2, s[40:43], s95 offen offset:0 sc1\n"
"buffer_load_dwordx4 v[100:103], v2, s[40:43], s95 offen offset:1024 sc1\n"
"s_waitcnt lgkmcnt(0)\n"
"v_mfma_f32_32x32x16_bf16 v[16:31], v[80:83], v[128:131], v[16:31]\n"
"ds_read_b64_tr_b16 v[88:89], v244 offset:10240\n"
"ds_read_b64_tr_b16 v[90:91], v244 offset:11264\n"
"ds_read_b64_tr_b16 v[92:93], v245 offset:10240\n"
"ds_read_b64_tr_b16 v[94:95], v245 offset:11264\n"
"v_mfma_f32_32x32x16_bf16 v[32:47], v[84:87], v[128:131], v[32:47]\n"
"v_mfma_f32_32x32x16_bf16 v[48:63], v[80:83], v[132:135], v[48:63]\n"
"v_mfma_f32_32x32x16_bf16 v[64:79], v[84:87], v[132:135], v[64:79]\n"
"buffer_load_dwordx4 v[104:107], v2, s[40:43], s95 offen offset:2048 sc1\n"
"buffer_load_dwordx4 v[108:111], v2, s[40:43], s95 offen offset:3072 sc1\n"
"buffer_load_dwordx4 v[112:115], v2, s[40:43], s84 offen offset:0 sc1\n"
"buffer_load_dwordx4 v[116:119], v2, s[40:43], s84 offen offset:1024 sc1\n"
"buffer_load_dwordx4 v[120:123], v2, s[40:43], s84 offen offset:2048 sc1\n"
"buffer_load_dwordx4 v[124:127], v2, s[40:43], s84 offen offset:3072 sc1\n"
"s_waitcnt lgkmcnt(0)\n"
"v_mfma_f32_32x32x16_bf16 v[16:31], v[88:91], v[136:139], v[16:31]\n"
"v_mfma_f32_32x32x16_bf16 v[32:47], v[92:95], v[136:139], v[32:47]\n"
"v_mfma_f32_32x32x16_bf16 v[48:63], v[88:91], v[140:143], v[48:63]\n"
"v_mfma_f32_32x32x16_bf16 v[64:79], v[92:95], v[140:143], v[64:79]\n"
"buffer_load_dwordx4 v[128:131], v2, s[40:43], s92 offen offset:0 sc1\n"
"buffer_load_dwordx4 v[132:135], v2, s[40:43], s92 offen offset:1024 sc1\n"
"buffer_load_dwordx4 v[136:139], v2, s[40:43], s92 offen offset:2048 sc1\n"
"buffer_load_dwordx4 v[140:143], v2, s[40:43], s92 offen offset:3072 sc1\n"
"v_lshlrev_b32 v4, 16, v224\n"
"v_lshlrev_b32 v5, 16, v10\n"
"v_add_f32 v232, v4, v5\n"
"v_and_b32 v4, 0xffff0000, v224\n"
"v_and_b32 v5, 0xffff0000, v10\n"
"v_add_f32 v233, v4, v5\n"
"v_lshlrev_b32 v4, 16, v225\n"
"v_lshlrev_b32 v5, 16, v11\n"
"v_add_f32 v234, v4, v5\n"
"v_and_b32 v4, 0xffff0000, v225\n"
"v_and_b32 v5, 0xffff0000, v11\n"
"v_add_f32 v235, v4, v5\n"
"v_lshlrev_b32 v4, 16, v226\n"
"v_lshlrev_b32 v5, 16, v12\n"
"v_add_f32 v236, v4, v5\n"
"v_and_b32 v4, 0xffff0000, v226\n"
"v_and_b32 v5, 0xffff0000, v12\n"
"v_add_f32 v237, v4, v5\n"
"v_lshlrev_b32 v4, 16, v227\n"
"v_lshlrev_b32 v5, 16, v13\n"
"v_add_f32 v238, v4, v5\n"
"v_and_b32 v4, 0xffff0000, v227\n"
"v_and_b32 v5, 0xffff0000, v13\n"
"v_add_f32 v239, v4, v5\n"
"v_mul_f32 v8, v15, v15\n"
"v_rcp_f32 v8, v8\n"
"s_waitcnt vmcnt(14)\n"
"v_max3_u32 v4, v144, v145, v146\n"
"v_max3_u32 v5, v147, v148, v149\n"
"v_max3_u32 v4, v4, v150, v151\n"
"v_max_u32 v4, v4, v5\n"
"v_cmp_eq_u32 vcc, -1, v4\n"
"s_nop 0\n"
"s_cbranch_vccz Look_120_%=\n"
"s_mov_b32 s76, 524288\n"
"Lospin_121_%=:\n"
"buffer_load_dwordx4 v[144:147], v2, s[40:43], s96 offen offset:0 sc1\n"
"buffer_load_dwordx4 v[148:151], v2, s[40:43], s96 offen offset:1024 sc1\n"
"s_waitcnt vmcnt(0)\n"
"v_max3_u32 v4, v144, v145, v146\n"
"v_max3_u32 v5, v147, v148, v149\n"
"v_max3_u32 v4, v4, v150, v151\n"
"v_max_u32 v4, v4, v5\n"
"v_cmp_eq_u32 vcc, -1, v4\n"
"s_sub_u32 s76, s76, 1\n"
"s_cbranch_vccz Look_120_%=\n"
"s_cmp_lg_u32 s76, 0\n"
"s_cbranch_scc1 Lospin_121_%=\n"
"Look_120_%=:\n"
"v_mfma_f32_32x32x16_bf16 v[16:31], v[160:163], v[144:147], v[16:31]\n"
"v_mfma_f32_32x32x16_bf16 v[32:47], v[164:167], v[144:147], v[32:47]\n"
"s_waitcnt vmcnt(12)\n"
"v_max3_u32 v4, v152, v153, v154\n"
"v_max3_u32 v5, v155, v156, v157\n"
"v_max3_u32 v4, v4, v158, v159\n"
"v_mfma_f32_32x32x16_bf16 v[16:31], v[168:171], v[148:151], v[16:31]\n"
"v_max_u32 v4, v4, v5\n"
"v_cmp_eq_u32 vcc, -1, v4\n"
"v_mfma_f32_32x32x16_bf16 v[32:47], v[172:175], v[148:151], v[32:47]\n"
"s_cbranch_vccz Look_122_%=\n"
"s_mov_b32 s76, 524288\n"
"Lospin_123_%=:\n"
"buffer_load_dwordx4 v[152:155], v2, s[40:43], s96 offen offset:2048 sc1\n"
"buffer_load_dwordx4 v[156:159], v2, s[40:43], s96 offen offset:3072 sc1\n"
"s_waitcnt vmcnt(0)\n"
"v_max3_u32 v4, v152, v153, v154\n"
"v_max3_u32 v5, v155, v156, v157\n"
"v_max3_u32 v4, v4, v158, v159\n"
"v_max_u32 v4, v4, v5\n"
"v_cmp_eq_u32 vcc, -1, v4\n"
"s_sub_u32 s76, s76, 1\n"
"s_cbranch_vccz Look_122_%=\n"
"s_cmp_lg_u32 s76, 0\n"
"s_cbranch_scc1 Lospin_123_%=\n"
"Look_122_%=:\n"
"v_mfma_f32_32x32x16_bf16 v[16:31], v[176:179], v[152:155], v[16:31]\n"
"v_mfma_f32_32x32x16_bf16 v[32:47], v[180:183], v[152:155], v[32:47]\n"
"s_waitcnt vmcnt(10)\n"
"v_max3_u32 v4, v96, v97, v98\n"
"v_max3_u32 v5, v99, v100, v101\n"
"v_max3_u32 v4, v4, v102, v103\n"
"v_mfma_f32_32x32x16_bf16 v[16:31], v[184:187], v[156:159], v[16:31]\n"
"v_max_u32 v4, v4, v5\n"
"v_cmp_eq_u32 vcc, -1, v4\n"
"v_mfma_f32_32x32x16_bf16 v[32:47], v[188:191], v[156:159], v[32:47]\n"
"s_cbranch_vccz Look_124_%=\n"
"s_mov_b32 s76, 524288\n"
"Lospin_125_%=:\n"
"buffer_load_dwordx4 v[96:99], v2, s[40:43], s95 offen offset:0 sc1\n"
"buffer_load_dwordx4 v[100:103], v2, s[40:43], s95 offen offset:1024 sc1\n"
"s_waitcnt vmcnt(0)\n"
"v_max3_u32 v4, v96, v97, v98\n"
"v_max3_u32 v5, v99, v100, v101\n"
"v_max3_u32 v4, v4, v102, v103\n"
"v_max_u32 v4, v4, v5\n"
"v_cmp_eq_u32 vcc, -1, v4\n"
"s_sub_u32 s76, s76, 1\n"
"s_cbranch_vccz Look_124_%=\n"
"s_cmp_lg_u32 s76, 0\n"
"s_cbranch_scc1 Lospin_125_%=\n"
"Look_124_%=:\n"
"v_mfma_f32_32x32x16_bf16 v[16:31], v[192:195], v[96:99], v[16:31]\n"
"v_mfma_f32_32x32x16_bf16 v[32:47], v[196:199], v[96:99], v[32:47]\n"
"s_waitcnt vmcnt(8)\n"
"v_max3_u32 v4, v104, v105, v106\n"
"v_max3_u32 v5, v107, v108, v109\n"
"v_max3_u32 v4, v4, v110, v111\n"
"v_mfma_f32_32x32x16_bf16 v[16:31], v[200:203], v[100:103], v[16:31]\n"
"v_max_u32 v4, v4, v5\n"
"v_cmp_eq_u32 vcc, -1, v4\n"
"v_mfma_f32_32x32x16_bf16 v[32:47], v[204:207], v[100:103], v[32:47]\n"
"s_cbranch_vccz Look_126_%=\n"
"s_mov_b32 s76, 524288\n"
"Lospin_127_%=:\n"
"buffer_load_dwordx4 v[104:107], v2, s[40:43], s95 offen offset:2048 sc1\n"
"buffer_load_dwordx4 v[108:111], v2, s[40:43], s95 offen offset:3072 sc1\n"
"s_waitcnt vmcnt(0)\n"
"v_max3_u32 v4, v104, v105, v106\n"
"v_max3_u32 v5, v107, v108, v109\n"
"v_max3_u32 v4, v4, v110, v111\n"
"v_max_u32 v4, v4, v5\n"
"v_cmp_eq_u32 vcc, -1, v4\n"
"s_sub_u32 s76, s76, 1\n"
"s_cbranch_vccz Look_126_%=\n"
"s_cmp_lg_u32 s76, 0\n"
"s_cbranch_scc1 Lospin_127_%=\n"
"Look_126_%=:\n"
"v_mfma_f32_32x32x16_bf16 v[16:31], v[208:211], v[104:107], v[16:31]\n"
"v_mfma_f32_32x32x16_bf16 v[32:47], v[212:215], v[104:107], v[32:47]\n"
"s_waitcnt vmcnt(6)\n"
"v_max3_u32 v4, v112, v113, v114\n"
"v_max3_u32 v5, v115, v116, v117\n"
"v_max3_u32 v4, v4, v118, v119\n"
"v_mfma_f32_32x32x16_bf16 v[16:31], v[216:219], v[108:111], v[16:31]\n"
"v_max_u32 v4, v4, v5\n"
"v_cmp_eq_u32 vcc, -1, v4\n"
"v_mfma_f32_32x32x16_bf16 v[32:47], v[220:223], v[108:111], v[32:47]\n"
"s_cbranch_vccz Look_128_%=\n"
"s_mov_b32 s76, 524288\n"
"Lospin_129_%=:\n"
"buffer_load_dwordx4 v[112:115], v2, s[40:43], s84 offen offset:0 sc1\n"
"buffer_load_dwordx4 v[116:119], v2, s[40:43], s84 offen offset:1024 sc1\n"
"s_waitcnt vmcnt(0)\n"
"v_max3_u32 v4, v112, v113, v114\n"
"v_max3_u32 v5, v115, v116, v117\n"
"v_max3_u32 v4, v4, v118, v119\n"
"v_max_u32 v4, v4, v5\n"
"v_cmp_eq_u32 vcc, -1, v4\n"
"s_sub_u32 s76, s76, 1\n"
"s_cbranch_vccz Look_128_%=\n"
"s_cmp_lg_u32 s76, 0\n"
"s_cbranch_scc1 Lospin_129_%=\n"
"Look_128_%=:\n"
"v_mfma_f32_32x32x16_bf16 v[48:63], v[160:163], v[112:115], v[48:63]\n"
"s_nop 6\n"
"ds_write_b128 v250, v[16:19] offset:0\n"
"v_mfma_f32_32x32x16_bf16 v[64:79], v[164:167], v[112:115], v[64:79]\n"
"s_waitcnt vmcnt(4)\n"
"v_max3_u32 v4, v120, v121, v122\n"
"v_max3_u32 v5, v123, v124, v125\n"
"v_max3_u32 v4, v4, v126, v127\n"
"v_mfma_f32_32x32x16_bf16 v[48:63], v[168:171], v[116:119], v[48:63]\n"
"ds_write_b128 v250, v[20:23] offset:1024\n"
"v_max_u32 v4, v4, v5\n"
"v_cmp_eq_u32 vcc, -1, v4\n"
"v_mfma_f32_32x32x16_bf16 v[64:79], v[172:175], v[116:119], v[64:79]\n"
"s_cbranch_vccz Look_130_%=\n"
"s_mov_b32 s76, 524288\n"
"Lospin_131_%=:\n"
"buffer_load_dwordx4 v[120:123], v2, s[40:43], s84 offen offset:2048 sc1\n"
"buffer_load_dwordx4 v[124:127], v2, s[40:43], s84 offen offset:3072 sc1\n"
"s_waitcnt vmcnt(0)\n"
"v_max3_u32 v4, v120, v121, v122\n"
"v_max3_u32 v5, v123, v124, v125\n"
"v_max3_u32 v4, v4, v126, v127\n"
"v_max_u32 v4, v4, v5\n"
"v_cmp_eq_u32 vcc, -1, v4\n"
"s_sub_u32 s76, s76, 1\n"
"s_cbranch_vccz Look_130_%=\n"
"s_cmp_lg_u32 s76, 0\n"
"s_cbranch_scc1 Lospin_131_%=\n"
"Look_130_%=:\n"
"v_mfma_f32_32x32x16_bf16 v[48:63], v[176:179], v[120:123], v[48:63]\n"
"ds_write_b128 v251, v[24:27] offset:0\n"
"v_mfma_f32_32x32x16_bf16 v[64:79], v[180:183], v[120:123], v[64:79]\n"
"s_waitcnt vmcnt(2)\n"
"v_max3_u32 v4, v128, v129, v130\n"
"v_max3_u32 v5, v131, v132, v133\n"
"v_max3_u32 v4, v4, v134, v135\n"
"v_mfma_f32_32x32x16_bf16 v[48:63], v[184:187], v[124:127], v[48:63]\n"
"ds_write_b128 v251, v[28:31] offset:1024\n"
"v_max_u32 v4, v4, v5\n"
"v_cmp_eq_u32 vcc, -1, v4\n"
"v_mfma_f32_32x32x16_bf16 v[64:79], v[188:191], v[124:127], v[64:79]\n"
"s_cbranch_vccz Look_132_%=\n"
"s_mov_b32 s76, 524288\n"
"Lospin_133_%=:\n"
"buffer_load_dwordx4 v[128:131], v2, s[40:43], s92 offen offset:0 sc1\n"
"buffer_load_dwordx4 v[132:135], v2, s[40:43], s92 offen offset:1024 sc1\n"
"s_waitcnt vmcnt(0)\n"
"v_max3_u32 v4, v128, v129, v130\n"
"v_max3_u32 v5, v131, v132, v133\n"
"v_max3_u32 v4, v4, v134, v135\n"
"v_max_u32 v4, v4, v5\n"
"v_cmp_eq_u32 vcc, -1, v4\n"
"s_sub_u32 s76, s76, 1\n"
"s_cbranch_vccz Look_132_%=\n"
"s_cmp_lg_u32 s76, 0\n"
"s_cbranch_scc1 Lospin_133_%=\n"
"Look_132_%=:\n"
"v_mfma_f32_32x32x16_bf16 v[48:63], v[192:195], v[128:131], v[48:63]\n"
"ds_write_b128 v252, v[32:35] offset:0\n"
"v_mfma_f32_32x32x16_bf16 v[64:79], v[196:199], v[128:131], v[64:79]\n"
"s_waitcnt vmcnt(0)\n"
"v_max3_u32 v4, v136, v137, v138\n"
"v_max3_u32 v5, v139, v140, v141\n"
"v_max3_u32 v4, v4, v142, v143\n"
"v_mfma_f32_32x32x16_bf16 v[48:63], v[200:203], v[132:135], v[48:63]\n"
"ds_write_b128 v252, v[36:39] offset:1024\n"
"v_max_u32 v4, v4, v5\n"
"v_cmp_eq_u32 vcc, -1, v4\n"
"v_mfma_f32_32x32x16_bf16 v[64:79], v[204:207], v[132:135], v[64:79]\n"
"s_cbranch_vccz Look_134_%=\n"
"s_mov_b32 s76, 524288\n"
"Lospin_135_%=:\n"
"buffer_load_dwordx4 v[136:139], v2, s[40:43], s92 offen offset:2048 sc1\n"
"buffer_load_dwordx4 v[140:143], v2, s[40:43], s92 offen offset:3072 sc1\n"
"s_waitcnt vmcnt(0)\n"
"v_max3_u32 v4, v136, v137, v138\n"
"v_max3_u32 v5, v139, v140, v141\n"
"v_max3_u32 v4, v4, v142, v143\n"
"v_max_u32 v4, v4, v5\n"
"v_cmp_eq_u32 vcc, -1, v4\n"
"s_sub_u32 s76, s76, 1\n"
"s_cbranch_vccz Look_134_%=\n"
"s_cmp_lg_u32 s76, 0\n"
"s_cbranch_scc1 Lospin_135_%=\n"
"Look_134_%=:\n"
"v_mfma_f32_32x32x16_bf16 v[64:79], v[212:215], v[136:139], v[64:79]\n"
"ds_write_b128 v253, v[40:43] offset:0\n"
"v_mfma_f32_32x32x16_bf16 v[64:79], v[220:223], v[140:143], v[64:79]\n"
"ds_write_b128 v253, v[44:47] offset:1024\n"
"v_mfma_f32_32x32x16_bf16 v[48:63], v[208:211], v[136:139], v[48:63]\n"
"v_mfma_f32_32x32x16_bf16 v[48:63], v[216:219], v[140:143], v[48:63]\n"
"s_nop 8\n"
"ds_write_b128 v252, v[64:67] offset:6144\n"
"ds_write_b128 v252, v[68:71] offset:7168\n"
"ds_write_b128 v253, v[72:75] offset:6144\n"
"ds_write_b128 v253, v[76:79] offset:7168\n"
"ds_write_b128 v251, v[56:59] offset:6144\n"
"ds_write_b128 v251, v[60:63] offset:7168\n"
"s_waitcnt lgkmcnt(0)\n"
"s_barrier\n"
"ds_read_b128 v[80:83], v250 offset:8192\n"
"ds_read_b128 v[84:87], v250 offset:9216\n"
"ds_read_b128 v[88:91], v250 offset:10240\n"
"ds_read_b128 v[92:95], v250 offset:11264\n"
"ds_read_b128 v[96:99], v250 offset:12288\n"
"ds_read_b128 v[100:103], v250 offset:13312\n"
"s_waitcnt lgkmcnt(4)\n"
"v_pk_add_f32 v[48:49], v[48:49], v[80:81]\n"
"v_pk_add_f32 v[50:51], v[50:51], v[82:83]\n"
"v_pk_add_f32 v[52:53], v[52:53], v[84:85]\n"
"v_pk_add_f32 v[54:55], v[54:55], v[86:87]\n"
"s_waitcnt lgkmcnt(2)\n"
"v_pk_add_f32 v[48:49], v[48:49], v[88:89]\n"
"v_pk_add_f32 v[50:51], v[50:51], v[90:91]\n"
"v_pk_add_f32 v[52:53], v[52:53], v[92:93]\n"
"v_pk_add_f32 v[54:55], v[54:55], v[94:95]\n"
"s_waitcnt lgkmcnt(0)\n"
"v_pk_add_f32 v[48:49], v[48:49], v[96:97]\n"
"v_pk_add_f32 v[50:51], v[50:51], v[98:99]\n"
"v_pk_add_f32 v[52:53], v[52:53], v[100:101]\n"
"v_pk_add_f32 v[54:55], v[54:55], v[102:103]\n"
"v_pk_fma_f32 v[48:49], v[48:49], v[8:9], v[232:233] op_sel_hi:[1,0,1]\n"
"v_pk_fma_f32 v[50:51], v[50:51], v[8:9], v[234:235] op_sel_hi:[1,0,1]\n"
"v_pk_fma_f32 v[52:53], v[52:53], v[8:9], v[236:237] op_sel_hi:[1,0,1]\n"
"v_pk_fma_f32 v[54:55], v[54:55], v[8:9], v[238:239] op_sel_hi:[1,0,1]\n"
"v_cvt_pk_bf16_f32 v104, v48, v49\n"
"v_cvt_pk_bf16_f32 v105, v50, v51\n"
"v_cvt_pk_bf16_f32 v106, v52, v53\n"
"v_cvt_pk_bf16_f32 v107, v54, v55\n"
"s_nop 0\n"
"v_permlane32_swap_b32 v104, v106\n"
"v_permlane32_swap_b32 v105, v107\n"
"buffer_store_dwordx4 v[104:107], v249, s[52:55], s87 offen\n"
"s_endpgm\n"
"Lbfin_116_%=:\n"
"v_pk_add_f32 v[100:101], v[100:101], v[168:169]\n"
"v_pk_add_f32 v[102:103], v[102:103], v[170:171]\n"
"v_pk_add_f32 v[104:105], v[104:105], v[172:173]\n"
"v_pk_add_f32 v[106:107], v[106:107], v[174:175]\n"
"v_add_f32 v108, v108, v184\n"
"s_nop 4\n"
"v_pk_add_f32 v[116:117], v[116:117], v[176:177]\n"
"v_pk_add_f32 v[118:119], v[118:119], v[178:179]\n"
"v_pk_add_f32 v[120:121], v[120:121], v[180:181]\n"
"v_pk_add_f32 v[122:123], v[122:123], v[182:183]\n"
"v_add_f32 v124, v124, v185\n"
"s_mov_b32 s74, 0x47800000\n"
"s_mov_b32 s75, 0x7f800000\n"
"s_mov_b32 s76, 0x02000000\n"
"v_maximum3_f32 v8, |v100|, |v101|, |v102|\n"
"v_maximum3_f32 v10, |v103|, |v104|, |v105|\n"
"v_maximum3_f32 v8, v8, |v106|, |v107|\n"
"v_maximum3_f32 v8, v8, v10, v10\n"
"v_maximum3_f32 v9, |v116|, |v117|, |v118|\n"
"v_maximum3_f32 v11, |v119|, |v120|, |v121|\n"
"v_maximum3_f32 v9, v9, |v122|, |v123|\n"
"v_maximum3_f32 v9, v9, v11, v11\n"
"v_maximum3_f32 v10, v108, v124, v124\n"
"v_mul_f32 v10, s74, v10\n"
"v_maximum3_f32 v10, v10, v8, v9\n"
"v_cmp_ngt_f32 vcc, s75, v10\n"
"v_min_f32 v8, v8, v9\n"
"v_cmp_gt_f32 s[30:31], s76, v8\n"
"s_nop 0\n"
"s_or_b64 vcc, vcc, s[30:31]\n"
"s_cbranch_vccnz Lfallback_136_%=\n"
"Lnorm_137_%=:\n"
"s_add_u32 s75, s86, 32768\n"
"v_rcp_f32 v4, v108\n"
"v_mul_f32 v8, v14, v14\n"
"v_mul_f32 v4, v4, v8\n"
"v_pk_mul_f32 v[100:101], v[100:101], v[4:5] op_sel_hi:[1,0]\n"
"v_pk_mul_f32 v[102:103], v[102:103], v[4:5] op_sel_hi:[1,0]\n"
"v_pk_mul_f32 v[104:105], v[104:105], v[4:5] op_sel_hi:[1,0]\n"
"v_pk_mul_f32 v[106:107], v[106:107], v[4:5] op_sel_hi:[1,0]\n"
"v_cvt_pk_bf16_f32 v16, v100, v101\n"
"v_cvt_pk_bf16_f32 v17, v102, v103\n"
"v_cvt_pk_bf16_f32 v18, v104, v105\n"
"v_cvt_pk_bf16_f32 v19, v106, v107\n"
"buffer_store_dwordx4 v[16:19], v2, s[40:43], s86 offen sc1\n"
"v_rcp_f32 v6, v124\n"
"v_mul_f32 v8, v15, v15\n"
"v_mul_f32 v6, v6, v8\n"
"v_pk_mul_f32 v[116:117], v[116:117], v[6:7] op_sel_hi:[1,0]\n"
"v_pk_mul_f32 v[118:119], v[118:119], v[6:7] op_sel_hi:[1,0]\n"
"v_pk_mul_f32 v[120:121], v[120:121], v[6:7] op_sel_hi:[1,0]\n"
"v_pk_mul_f32 v[122:123], v[122:123], v[6:7] op_sel_hi:[1,0]\n"
"v_cvt_pk_bf16_f32 v20, v116, v117\n"
"v_cvt_pk_bf16_f32 v21, v118, v119\n"
"v_cvt_pk_bf16_f32 v22, v120, v121\n"
"v_cvt_pk_bf16_f32 v23, v122, v123\n"
"buffer_store_dwordx4 v[20:23], v2, s[40:43], s75 offen sc1\n"
"s_barrier\n"
"s_waitcnt vmcnt(2)\n"
"v_lshlrev_b32 v4, 16, v188\n"
"v_lshlrev_b32 v5, 16, v192\n"
"v_add_f32 v196, v4, v5\n"
"v_and_b32 v4, 0xffff0000, v188\n"
"v_and_b32 v5, 0xffff0000, v192\n"
"v_add_f32 v197, v4, v5\n"
"v_lshlrev_b32 v4, 16, v189\n"
"v_lshlrev_b32 v5, 16, v193\n"
"v_add_f32 v198, v4, v5\n"
"v_and_b32 v4, 0xffff0000, v189\n"
"v_and_b32 v5, 0xffff0000, v193\n"
"v_add_f32 v199, v4, v5\n"
"v_lshlrev_b32 v4, 16, v190\n"
"v_lshlrev_b32 v5, 16, v194\n"
"v_add_f32 v200, v4, v5\n"
"v_and_b32 v4, 0xffff0000, v190\n"
"v_and_b32 v5, 0xffff0000, v194\n"
"v_add_f32 v201, v4, v5\n"
"v_lshlrev_b32 v4, 16, v191\n"
"v_lshlrev_b32 v5, 16, v195\n"
"v_add_f32 v202, v4, v5\n"
"v_and_b32 v4, 0xffff0000, v191\n"
"v_and_b32 v5, 0xffff0000, v195\n"
"v_add_f32 v203, v4, v5\n"
"v_mul_f32 v8, v14, v14\n"
"v_rcp_f32 v8, v8\n"
"s_barrier\n"
"ds_read_b128 v[32:35], v250 offset:0\n"
"ds_read_b128 v[36:39], v250 offset:1024\n"
"ds_read_b128 v[40:43], v250 offset:2048\n"
"ds_read_b128 v[44:47], v250 offset:3072\n"
"ds_read_b128 v[48:51], v250 offset:4096\n"
"ds_read_b128 v[52:55], v250 offset:5120\n"
"ds_read_b128 v[56:59], v250 offset:6144\n"
"ds_read_b128 v[60:63], v250 offset:7168\n"
"s_waitcnt lgkmcnt(4)\n"
"v_pk_add_f32 v[32:33], v[32:33], v[40:41]\n"
"v_pk_add_f32 v[34:35], v[34:35], v[42:43]\n"
"v_pk_add_f32 v[36:37], v[36:37], v[44:45]\n"
"v_pk_add_f32 v[38:39], v[38:39], v[46:47]\n"
"s_waitcnt lgkmcnt(2)\n"
"v_pk_add_f32 v[32:33], v[32:33], v[48:49]\n"
"v_pk_add_f32 v[34:35], v[34:35], v[50:51]\n"
"v_pk_add_f32 v[36:37], v[36:37], v[52:53]\n"
"v_pk_add_f32 v[38:39], v[38:39], v[54:55]\n"
"s_waitcnt lgkmcnt(0)\n"
"v_pk_add_f32 v[32:33], v[32:33], v[56:57]\n"
"v_pk_add_f32 v[34:35], v[34:35], v[58:59]\n"
"v_pk_add_f32 v[36:37], v[36:37], v[60:61]\n"
"v_pk_add_f32 v[38:39], v[38:39], v[62:63]\n"
"v_pk_fma_f32 v[32:33], v[32:33], v[8:9], v[196:197] op_sel_hi:[1,0,1]\n"
"v_pk_fma_f32 v[34:35], v[34:35], v[8:9], v[198:199] op_sel_hi:[1,0,1]\n"
"v_pk_fma_f32 v[36:37], v[36:37], v[8:9], v[200:201] op_sel_hi:[1,0,1]\n"
"v_pk_fma_f32 v[38:39], v[38:39], v[8:9], v[202:203] op_sel_hi:[1,0,1]\n"
"v_cvt_pk_bf16_f32 v64, v32, v33\n"
"v_cvt_pk_bf16_f32 v65, v34, v35\n"
"v_cvt_pk_bf16_f32 v66, v36, v37\n"
"v_cvt_pk_bf16_f32 v67, v38, v39\n"
"s_nop 0\n"
"v_permlane32_swap_b32 v64, v66\n"
"v_permlane32_swap_b32 v65, v67\n"
"buffer_store_dwordx4 v[64:67], v249, s[52:55], s87 offen\n"
"s_endpgm\n"
"Lfallback_136_%=:\n"
"s_nop 15\n"
"s_nop 3\n"
"v_mov_b32 v4, 0xff800000\n"
"v_mov_b32 v6, 0xff800000\n"
"v_mov_b32 v7, v238\n"
"v_mov_b32 v168, v148\n"
"v_mov_b32 v169, v148\n"
"v_mov_b32 v170, v148\n"
"v_mov_b32 v171, v148\n"
"v_mov_b32 v172, v148\n"
"v_mov_b32 v173, v148\n"
"v_mov_b32 v174, v148\n"
"v_mov_b32 v175, v148\n"
"v_mov_b32 v176, v148\n"
"v_mov_b32 v177, v148\n"
"v_mov_b32 v178, v148\n"
"v_mov_b32 v179, v148\n"
"v_mov_b32 v180, v148\n"
"v_mov_b32 v181, v148\n"
"v_mov_b32 v182, v148\n"
"v_mov_b32 v183, v148\n"
"v_mov_b32 v196, v148\n"
"v_mov_b32 v197, v148\n"
"v_mov_b32 v198, v148\n"
"v_mov_b32 v199, v148\n"
"v_mov_b32 v200, v148\n"
"v_mov_b32 v201, v148\n"
"v_mov_b32 v202, v148\n"
"v_mov_b32 v203, v148\n"
"v_mov_b32 v204, v148\n"
"v_mov_b32 v205, v148\n"
"v_mov_b32 v206, v148\n"
"v_mov_b32 v207, v148\n"
"v_mov_b32 v208, v148\n"
"v_mov_b32 v209, v148\n"
"v_mov_b32 v210, v148\n"
"v_mov_b32 v211, v148\n"
"s_mov_b32 s74, 0\n"
"s_mov_b32 s75, 4096\n"
"s_nop 15\n"
"s_nop 3\n"
"Lfb_138_%=:\n"
"buffer_load_dwordx4 v[80:83], v2, s[40:43], s74 offen offset:0 sc1\n"
"buffer_load_dwordx4 v[84:87], v2, s[40:43], s74 offen offset:2048 sc1\n"
"buffer_load_dwordx4 v[88:91], v2, s[40:43], s75 offen offset:0 sc1\n"
"buffer_load_dwordx4 v[92:95], v2, s[40:43], s75 offen offset:2048 sc1\n"
"s_waitcnt vmcnt(0)\n"
"v_mfma_f32_32x32x16_bf16 v[16:31], v[80:83], v[132:135], 0\n"
"v_mfma_f32_32x32x16_bf16 v[32:47], v[80:83], v[136:139], 0\n"
"s_nop 10\n"
"v_max3_f32 v4, v4, v16, v17\n"
"v_max3_f32 v4, v4, v18, v19\n"
"v_max3_f32 v4, v4, v20, v21\n"
"v_max3_f32 v4, v4, v22, v23\n"
"v_max3_f32 v4, v4, v24, v25\n"
"v_max3_f32 v4, v4, v26, v27\n"
"v_max3_f32 v4, v4, v28, v29\n"
"v_max3_f32 v4, v4, v30, v31\n"
"v_max3_f32 v6, v6, v32, v33\n"
"v_max3_f32 v6, v6, v34, v35\n"
"v_max3_f32 v6, v6, v36, v37\n"
"v_max3_f32 v6, v6, v38, v39\n"
"v_max3_f32 v6, v6, v40, v41\n"
"v_max3_f32 v6, v6, v42, v43\n"
"v_max3_f32 v6, v6, v44, v45\n"
"v_max3_f32 v6, v6, v46, v47\n"
"v_mfma_f32_32x32x16_bf16 v[16:31], v[84:87], v[132:135], 0\n"
"v_mfma_f32_32x32x16_bf16 v[32:47], v[84:87], v[136:139], 0\n"
"s_nop 10\n"
"v_max3_f32 v4, v4, v16, v17\n"
"v_max3_f32 v4, v4, v18, v19\n"
"v_max3_f32 v4, v4, v20, v21\n"
"v_max3_f32 v4, v4, v22, v23\n"
"v_max3_f32 v4, v4, v24, v25\n"
"v_max3_f32 v4, v4, v26, v27\n"
"v_max3_f32 v4, v4, v28, v29\n"
"v_max3_f32 v4, v4, v30, v31\n"
"v_max3_f32 v6, v6, v32, v33\n"
"v_max3_f32 v6, v6, v34, v35\n"
"v_max3_f32 v6, v6, v36, v37\n"
"v_max3_f32 v6, v6, v38, v39\n"
"v_max3_f32 v6, v6, v40, v41\n"
"v_max3_f32 v6, v6, v42, v43\n"
"v_max3_f32 v6, v6, v44, v45\n"
"v_max3_f32 v6, v6, v46, v47\n"
"v_mfma_f32_32x32x16_bf16 v[16:31], v[88:91], v[132:135], 0\n"
"v_mfma_f32_32x32x16_bf16 v[32:47], v[88:91], v[136:139], 0\n"
"s_nop 10\n"
"v_max3_f32 v4, v4, v16, v17\n"
"v_max3_f32 v4, v4, v18, v19\n"
"v_max3_f32 v4, v4, v20, v21\n"
"v_max3_f32 v4, v4, v22, v23\n"
"v_max3_f32 v4, v4, v24, v25\n"
"v_max3_f32 v4, v4, v26, v27\n"
"v_max3_f32 v4, v4, v28, v29\n"
"v_max3_f32 v4, v4, v30, v31\n"
"v_max3_f32 v6, v6, v32, v33\n"
"v_max3_f32 v6, v6, v34, v35\n"
"v_max3_f32 v6, v6, v36, v37\n"
"v_max3_f32 v6, v6, v38, v39\n"
"v_max3_f32 v6, v6, v40, v41\n"
"v_max3_f32 v6, v6, v42, v43\n"
"v_max3_f32 v6, v6, v44, v45\n"
"v_max3_f32 v6, v6, v46, v47\n"
"v_mfma_f32_32x32x16_bf16 v[16:31], v[92:95], v[132:135], 0\n"
"v_mfma_f32_32x32x16_bf16 v[32:47], v[92:95], v[136:139], 0\n"
"s_nop 10\n"
"v_max3_f32 v4, v4, v16, v17\n"
"v_max3_f32 v4, v4, v18, v19\n"
"v_max3_f32 v4, v4, v20, v21\n"
"v_max3_f32 v4, v4, v22, v23\n"
"v_max3_f32 v4, v4, v24, v25\n"
"v_max3_f32 v4, v4, v26, v27\n"
"v_max3_f32 v4, v4, v28, v29\n"
"v_max3_f32 v4, v4, v30, v31\n"
"v_max3_f32 v6, v6, v32, v33\n"
"v_max3_f32 v6, v6, v34, v35\n"
"v_max3_f32 v6, v6, v36, v37\n"
"v_max3_f32 v6, v6, v38, v39\n"
"v_max3_f32 v6, v6, v40, v41\n"
"v_max3_f32 v6, v6, v42, v43\n"
"v_max3_f32 v6, v6, v44, v45\n"
"v_max3_f32 v6, v6, v46, v47\n"
"s_nop 15\n"
"s_nop 3\n"
"s_add_u32 s74, s74, 8192\n"
"s_add_u32 s75, s75, 8192\n"
"s_cmp_lt_u32 s74, 131072\n"
"s_cbranch_scc1 Lfb_138_%=\n"
"v_mov_b32 v5, v4\n"
"s_nop 1\n"
"v_permlane32_swap_b32 v4, v5\n"
"v_max_f32 v4, v4, v5\n"
"v_mov_b32 v5, v6\n"
"s_nop 1\n"
"v_permlane32_swap_b32 v6, v5\n"
"v_max_f32 v6, v6, v5\n"
"v_sub_f32 v48, 0, v4\n"
"v_mov_b32 v49, v48\n"
"v_pk_mov_b32 v[50:51], v[48:49], v[48:49]\n"
"v_pk_mov_b32 v[52:53], v[48:49], v[48:49]\n"
"v_pk_mov_b32 v[54:55], v[48:49], v[48:49]\n"
"v_pk_mov_b32 v[56:57], v[48:49], v[48:49]\n"
"v_pk_mov_b32 v[58:59], v[48:49], v[48:49]\n"
"v_pk_mov_b32 v[60:61], v[48:49], v[48:49]\n"
"v_pk_mov_b32 v[62:63], v[48:49], v[48:49]\n"
"v_sub_f32 v64, 0, v6\n"
"v_mov_b32 v65, v64\n"
"v_pk_mov_b32 v[66:67], v[64:65], v[64:65]\n"
"v_pk_mov_b32 v[68:69], v[64:65], v[64:65]\n"
"v_pk_mov_b32 v[70:71], v[64:65], v[64:65]\n"
"v_pk_mov_b32 v[72:73], v[64:65], v[64:65]\n"
"v_pk_mov_b32 v[74:75], v[64:65], v[64:65]\n"
"v_pk_mov_b32 v[76:77], v[64:65], v[64:65]\n"
"v_pk_mov_b32 v[78:79], v[64:65], v[64:65]\n"
"v_mov_b32 v100, 0\n"
"v_mov_b32 v101, 0\n"
"v_mov_b32 v102, 0\n"
"v_mov_b32 v103, 0\n"
"v_mov_b32 v104, 0\n"
"v_mov_b32 v105, 0\n"
"v_mov_b32 v106, 0\n"
"v_mov_b32 v107, 0\n"
"v_mov_b32 v108, 0\n"
"v_mov_b32 v109, 0\n"
"v_mov_b32 v110, 0\n"
"v_mov_b32 v111, 0\n"
"v_mov_b32 v112, 0\n"
"v_mov_b32 v113, 0\n"
"v_mov_b32 v114, 0\n"
"v_mov_b32 v115, 0\n"
"v_mov_b32 v116, 0\n"
"v_mov_b32 v117, 0\n"
"v_mov_b32 v118, 0\n"
"v_mov_b32 v119, 0\n"
"v_mov_b32 v120, 0\n"
"v_mov_b32 v121, 0\n"
"v_mov_b32 v122, 0\n"
"v_mov_b32 v123, 0\n"
"v_mov_b32 v124, 0\n"
"v_mov_b32 v125, 0\n"
"v_mov_b32 v126, 0\n"
"v_mov_b32 v127, 0\n"
"v_mov_b32 v128, 0\n"
"v_mov_b32 v129, 0\n"
"v_mov_b32 v130, 0\n"
"v_mov_b32 v131, 0\n"
"s_mov_b32 s74, 0\n"
"s_mov_b32 s75, 4096\n"
"s_nop 15\n"
"s_nop 3\n"
"Lfb_139_%=:\n"
"buffer_load_dwordx4 v[80:83], v2, s[40:43], s74 offen offset:0 sc1\n"
"buffer_load_dwordx4 v[84:87], v2, s[40:43], s74 offen offset:2048 sc1\n"
"buffer_load_dwordx4 v[88:91], v2, s[40:43], s75 offen offset:0 sc1\n"
"buffer_load_dwordx4 v[92:95], v2, s[40:43], s75 offen offset:2048 sc1\n"
"s_mov_b64 exec, s[90:91]\n"
"buffer_load_dwordx4 v[168:171], v7, s[40:43], s74 offen offset:0 sc1\n"
"buffer_load_dwordx4 v[172:175], v7, s[40:43], s74 offen offset:512 sc1\n"
"buffer_load_dwordx4 v[176:179], v7, s[40:43], s74 offen offset:2048 sc1\n"
"buffer_load_dwordx4 v[180:183], v7, s[40:43], s74 offen offset:2560 sc1\n"
"buffer_load_dwordx4 v[196:199], v7, s[40:43], s75 offen offset:0 sc1\n"
"buffer_load_dwordx4 v[200:203], v7, s[40:43], s75 offen offset:512 sc1\n"
"buffer_load_dwordx4 v[204:207], v7, s[40:43], s75 offen offset:2048 sc1\n"
"buffer_load_dwordx4 v[208:211], v7, s[40:43], s75 offen offset:2560 sc1\n"
"s_mov_b64 exec, -1\n"
"s_waitcnt vmcnt(0)\n"
"v_mfma_f32_32x32x16_bf16 v[16:31], v[80:83], v[132:135], v[48:63]\n"
"v_mfma_f32_32x32x16_bf16 v[32:47], v[80:83], v[136:139], v[64:79]\n"
"s_nop 10\n"
"v_exp_f32 v16, v16\n"
"v_exp_f32 v17, v17\n"
"v_exp_f32 v18, v18\n"
"v_exp_f32 v19, v19\n"
"v_exp_f32 v20, v20\n"
"v_exp_f32 v21, v21\n"
"v_exp_f32 v22, v22\n"
"v_exp_f32 v23, v23\n"
"v_exp_f32 v24, v24\n"
"v_exp_f32 v25, v25\n"
"v_exp_f32 v26, v26\n"
"v_exp_f32 v27, v27\n"
"v_exp_f32 v28, v28\n"
"v_exp_f32 v29, v29\n"
"v_exp_f32 v30, v30\n"
"v_exp_f32 v31, v31\n"
"v_smfmac_f32_32x32x32_bf16 v[100:115], v[168:171], v[16:23], v164\n"
"s_nop 0\n"
"v_smfmac_f32_32x32x32_bf16 v[100:115], v[172:175], v[24:31], v164\n"
"v_exp_f32 v32, v32\n"
"v_exp_f32 v33, v33\n"
"v_exp_f32 v34, v34\n"
"v_exp_f32 v35, v35\n"
"v_exp_f32 v36, v36\n"
"v_exp_f32 v37, v37\n"
"v_exp_f32 v38, v38\n"
"v_exp_f32 v39, v39\n"
"v_exp_f32 v40, v40\n"
"v_exp_f32 v41, v41\n"
"v_exp_f32 v42, v42\n"
"v_exp_f32 v43, v43\n"
"v_exp_f32 v44, v44\n"
"v_exp_f32 v45, v45\n"
"v_exp_f32 v46, v46\n"
"v_exp_f32 v47, v47\n"
"v_smfmac_f32_32x32x32_bf16 v[116:131], v[168:171], v[32:39], v164\n"
"s_nop 0\n"
"v_smfmac_f32_32x32x32_bf16 v[116:131], v[172:175], v[40:47], v164\n"
"v_mfma_f32_32x32x16_bf16 v[16:31], v[84:87], v[132:135], v[48:63]\n"
"v_mfma_f32_32x32x16_bf16 v[32:47], v[84:87], v[136:139], v[64:79]\n"
"s_nop 10\n"
"v_exp_f32 v16, v16\n"
"v_exp_f32 v17, v17\n"
"v_exp_f32 v18, v18\n"
"v_exp_f32 v19, v19\n"
"v_exp_f32 v20, v20\n"
"v_exp_f32 v21, v21\n"
"v_exp_f32 v22, v22\n"
"v_exp_f32 v23, v23\n"
"v_exp_f32 v24, v24\n"
"v_exp_f32 v25, v25\n"
"v_exp_f32 v26, v26\n"
"v_exp_f32 v27, v27\n"
"v_exp_f32 v28, v28\n"
"v_exp_f32 v29, v29\n"
"v_exp_f32 v30, v30\n"
"v_exp_f32 v31, v31\n"
"v_smfmac_f32_32x32x32_bf16 v[100:115], v[176:179], v[16:23], v164\n"
"s_nop 0\n"
"v_smfmac_f32_32x32x32_bf16 v[100:115], v[180:183], v[24:31], v164\n"
"v_exp_f32 v32, v32\n"
"v_exp_f32 v33, v33\n"
"v_exp_f32 v34, v34\n"
"v_exp_f32 v35, v35\n"
"v_exp_f32 v36, v36\n"
"v_exp_f32 v37, v37\n"
"v_exp_f32 v38, v38\n"
"v_exp_f32 v39, v39\n"
"v_exp_f32 v40, v40\n"
"v_exp_f32 v41, v41\n"
"v_exp_f32 v42, v42\n"
"v_exp_f32 v43, v43\n"
"v_exp_f32 v44, v44\n"
"v_exp_f32 v45, v45\n"
"v_exp_f32 v46, v46\n"
"v_exp_f32 v47, v47\n"
"v_smfmac_f32_32x32x32_bf16 v[116:131], v[176:179], v[32:39], v164\n"
"s_nop 0\n"
"v_smfmac_f32_32x32x32_bf16 v[116:131], v[180:183], v[40:47], v164\n"
"v_mfma_f32_32x32x16_bf16 v[16:31], v[88:91], v[132:135], v[48:63]\n"
"v_mfma_f32_32x32x16_bf16 v[32:47], v[88:91], v[136:139], v[64:79]\n"
"s_nop 10\n"
"v_exp_f32 v16, v16\n"
"v_exp_f32 v17, v17\n"
"v_exp_f32 v18, v18\n"
"v_exp_f32 v19, v19\n"
"v_exp_f32 v20, v20\n"
"v_exp_f32 v21, v21\n"
"v_exp_f32 v22, v22\n"
"v_exp_f32 v23, v23\n"
"v_exp_f32 v24, v24\n"
"v_exp_f32 v25, v25\n"
"v_exp_f32 v26, v26\n"
"v_exp_f32 v27, v27\n"
"v_exp_f32 v28, v28\n"
"v_exp_f32 v29, v29\n"
"v_exp_f32 v30, v30\n"
"v_exp_f32 v31, v31\n"
"v_smfmac_f32_32x32x32_bf16 v[100:115], v[196:199], v[16:23], v164\n"
"s_nop 0\n"
"v_smfmac_f32_32x32x32_bf16 v[100:115], v[200:203], v[24:31], v164\n"
"v_exp_f32 v32, v32\n"
"v_exp_f32 v33, v33\n"
"v_exp_f32 v34, v34\n"
"v_exp_f32 v35, v35\n"
"v_exp_f32 v36, v36\n"
"v_exp_f32 v37, v37\n"
"v_exp_f32 v38, v38\n"
"v_exp_f32 v39, v39\n"
"v_exp_f32 v40, v40\n"
"v_exp_f32 v41, v41\n"
"v_exp_f32 v42, v42\n"
"v_exp_f32 v43, v43\n"
"v_exp_f32 v44, v44\n"
"v_exp_f32 v45, v45\n"
"v_exp_f32 v46, v46\n"
"v_exp_f32 v47, v47\n"
"v_smfmac_f32_32x32x32_bf16 v[116:131], v[196:199], v[32:39], v164\n"
"s_nop 0\n"
"v_smfmac_f32_32x32x32_bf16 v[116:131], v[200:203], v[40:47], v164\n"
"v_mfma_f32_32x32x16_bf16 v[16:31], v[92:95], v[132:135], v[48:63]\n"
"v_mfma_f32_32x32x16_bf16 v[32:47], v[92:95], v[136:139], v[64:79]\n"
"s_nop 10\n"
"v_exp_f32 v16, v16\n"
"v_exp_f32 v17, v17\n"
"v_exp_f32 v18, v18\n"
"v_exp_f32 v19, v19\n"
"v_exp_f32 v20, v20\n"
"v_exp_f32 v21, v21\n"
"v_exp_f32 v22, v22\n"
"v_exp_f32 v23, v23\n"
"v_exp_f32 v24, v24\n"
"v_exp_f32 v25, v25\n"
"v_exp_f32 v26, v26\n"
"v_exp_f32 v27, v27\n"
"v_exp_f32 v28, v28\n"
"v_exp_f32 v29, v29\n"
"v_exp_f32 v30, v30\n"
"v_exp_f32 v31, v31\n"
"v_smfmac_f32_32x32x32_bf16 v[100:115], v[204:207], v[16:23], v164\n"
"s_nop 0\n"
"v_smfmac_f32_32x32x32_bf16 v[100:115], v[208:211], v[24:31], v164\n"
"v_exp_f32 v32, v32\n"
"v_exp_f32 v33, v33\n"
"v_exp_f32 v34, v34\n"
"v_exp_f32 v35, v35\n"
"v_exp_f32 v36, v36\n"
"v_exp_f32 v37, v37\n"
"v_exp_f32 v38, v38\n"
"v_exp_f32 v39, v39\n"
"v_exp_f32 v40, v40\n"
"v_exp_f32 v41, v41\n"
"v_exp_f32 v42, v42\n"
"v_exp_f32 v43, v43\n"
"v_exp_f32 v44, v44\n"
"v_exp_f32 v45, v45\n"
"v_exp_f32 v46, v46\n"
"v_exp_f32 v47, v47\n"
"v_smfmac_f32_32x32x32_bf16 v[116:131], v[204:207], v[32:39], v164\n"
"s_nop 0\n"
"v_smfmac_f32_32x32x32_bf16 v[116:131], v[208:211], v[40:47], v164\n"
"s_nop 15\n"
"s_nop 3\n"
"s_add_u32 s74, s74, 8192\n"
"s_add_u32 s75, s75, 8192\n"
"s_cmp_lt_u32 s74, 131072\n"
"s_cbranch_scc1 Lfb_139_%=\n"
"s_nop 15\n"
"s_nop 3\n"
"s_branch Lnorm_137_%=\n"
"Lend_2_%=:\n"
        :
        : "s"(__builtin_amdgcn_kernarg_segment_ptr()), "s"(e_lo), "s"(e_hi), "s"(blockIdx.x), "v"(threadIdx.x),
          "s"((unsigned)(unsigned long long)(__attribute__((address_space(3))) char*)smem)
        : "v1", "v2", "v3", "v4", "v5", "v6", "v7", "v8", "v9", "v10", "v11", "v12", "v13", "v14", "v15", "v16", "v17", "v18", "v19", "v20", "v21", "v22", "v23", "v24", "v25", "v26", "v27", "v28", "v29", "v30", "v31", "v32", "v33", "v34", "v35", "v36", "v37", "v38", "v39", "v40", "v41", "v42", "v43", "v44", "v45", "v46", "v47", "v48", "v49", "v50", "v51", "v52", "v53", "v54", "v55", "v56", "v57", "v58", "v59", "v60", "v61", "v62", "v63", "v64", "v65", "v66", "v67", "v68", "v69", "v70", "v71", "v72", "v73", "v74", "v75", "v76", "v77", "v78", "v79", "v80", "v81", "v82", "v83", "v84", "v85", "v86", "v87", "v88", "v89", "v90", "v91", "v92", "v93", "v94", "v95", "v96", "v97", "v98", "v99", "v100", "v101", "v102", "v103", "v104", "v105", "v106", "v107", "v108", "v109", "v110", "v111", "v112", "v113", "v114", "v115", "v116", "v117", "v118", "v119", "v120", "v121", "v122", "v123", "v124", "v125", "v126", "v127", "v128", "v129", "v130", "v131", "v132", "v133", "v134", "v135", "v136", "v137", "v138", "v139", "v140", "v141", "v142", "v143", "v144", "v145", "v146", "v147", "v148", "v149", "v150", "v151", "v152", "v153", "v154", "v155", "v156", "v157", "v158", "v159", "v160", "v161", "v162", "v163", "v164", "v165", "v166", "v167", "v168", "v169", "v170", "v171", "v172", "v173", "v174", "v175", "v176", "v177", "v178", "v179", "v180", "v181", "v182", "v183", "v184", "v185", "v186", "v187", "v188", "v189", "v190", "v191", "v192", "v193", "v194", "v195", "v196", "v197", "v198", "v199", "v200", "v201", "v202", "v203", "v204", "v205", "v206", "v207", "v208", "v209", "v210", "v211", "v212", "v213", "v214", "v215", "v216", "v217", "v218", "v219", "v220", "v221", "v222", "v223", "v224", "v225", "v226", "v227", "v228", "v229", "v230", "v231", "v232", "v233", "v234", "v235", "v236", "v237", "v238", "v239", "v240", "v241", "v242", "v243", "v244", "v245", "v246", "v247", "v248", "v249", "v250", "v251", "v252", "v253", "v254", "v255", "s8", "s9", "s10", "s11", "s12", "s13", "s14", "s15", "s16", "s17", "s18", "s19", "s20", "s21", "s22", "s23", "s24", "s25", "s26", "s27", "s28", "s29", "s30", "s31", "s33", "s34", "s35", "s36", "s37", "s38", "s39", "s40", "s41", "s42", "s43", "s44", "s45", "s46", "s47", "s48", "s49", "s50", "s51", "s52", "s53", "s54", "s55", "s56", "s57", "s58", "s59", "s60", "s61", "s62", "s63", "s64", "s65", "s66", "s67", "s68", "s69", "s70", "s71", "s72", "s73", "s74", "s75", "s76", "s77", "s78", "s79", "s80", "s81", "s82", "s83", "s84", "s85", "s86", "s87", "s88", "s89", "s90", "s91", "s92", "s93", "s94", "s95", "s96", "s97", "s98", "s99", "vcc", "memory");
}

extern "C" void kernel_launch(void* const* d_in, const int* in_sizes, int n_in, void* d_out, int out_size, void* d_ws,
                              size_t ws_size, hipStream_t stream) {
    fused_mqa_ffn_kernel<<<256, 512, 0, stream>>>(d_in[0], d_in[1], d_in[2], d_in[3], d_in[4], d_in[6], d_in[7], d_in[8],
                                                  d_in[9], d_out, d_ws);
}
```
